```python
import torch

def _softmax(x, axis=-1):
    return torch.softmax(x, dim=axis)
def _gelu(x):
    return torch.nn.functional.gelu(x, approximate="tanh")
def _silu(x):
    return torch.nn.functional.silu(x)
def _sigmoid(x):
    return torch.sigmoid(x)
def _layernorm(x, axis=-1, eps=1e-5):
    m = x.mean(dim=axis, keepdim=True)
    v = x.var(dim=axis, keepdim=True, unbiased=False)
    return (x - m) / torch.sqrt(v + eps)
def _scatter_add(tgt, idx, src, axis=0):
    return tgt.clone().index_add_(axis, idx, src)
def _segment_sum(src, seg, n):
    out = torch.zeros((n,) + src.shape[1:], dtype=src.dtype)
    return out.index_add_(0, seg, src)
def _pad(x, pw):
    flat = tuple(v for pair in reversed(pw) for v in pair)
    return torch.nn.functional.pad(x, flat)

kernel_name = 'concat_heads__gqa_block_cached__sliding_block_cached__decode'

def setup_inputs():

    _w = (lambda a: (lambda t: t.to(torch.bfloat16) if t.is_floating_point() else t)(torch.as_tensor(a)))
    def _A_setup():
        B, Sq, D = 1, 1, 640
        Hq, Hkv, Sk, Dh = 32, 32, 8192, 128
        assert Hq % Hkv == 0, (Hq, Hkv)
        return {
            "x": _w((lambda *s: torch.randn(*s, dtype=torch.bfloat16) * 0.8)(B, Sq, D)),
            "k_cache": _w((lambda *s: torch.randn(*s, dtype=torch.bfloat16) * 1.6)(B, Hkv, Sk, Dh)),
            "v_cache": _w((lambda *s: torch.randn(*s, dtype=torch.bfloat16) * 0.2)(B, Hkv, Sk, Dh)),
            "wq": _w((lambda *s: torch.randn(*s, dtype=torch.bfloat16) * 1.6)(D, Hq * Dh) * (D ** -0.5)),
            "wo": _w((lambda *s: torch.randn(*s, dtype=torch.bfloat16) * 0.2)(Hq * Dh, D) * ((Hq * Dh) ** -0.5)),
        }
    _A_in = _A_setup()

    def _B_setup():
        B, Sq, D = 1, 1, 640
        Hq, Hkv, Sk, Dh = 32, 32, 8192, 128
        assert Hq % Hkv == 0, (Hq, Hkv)
        return {
            "x": _w((lambda *s: torch.randn(*s, dtype=torch.bfloat16) * 0.8)(B, Sq, D)),
            "k_cache": _w((lambda *s: torch.randn(*s, dtype=torch.bfloat16) * 1.6)(B, Hkv, Sk, Dh)),
            "v_cache": _w((lambda *s: torch.randn(*s, dtype=torch.bfloat16) * 0.2)(B, Hkv, Sk, Dh)),
            "wq": _w((lambda *s: torch.randn(*s, dtype=torch.bfloat16) * 1.6)(D, Hq * Dh) * (D ** -0.5)),
            "wo": _w((lambda *s: torch.randn(*s, dtype=torch.bfloat16) * 0.2)(Hq * Dh, D) * ((Hq * Dh) ** -0.5)),
        }
    _B_in = _B_setup()

    return {
        "A_x": _A_in["x"],
        "A_k_cache": _A_in["k_cache"],
        "A_v_cache": _A_in["v_cache"],
        "A_wq": _A_in["wq"],
        "A_wo": _A_in["wo"],
        "B_x": _B_in["x"],
        "B_k_cache": _B_in["k_cache"],
        "B_v_cache": _B_in["v_cache"],
        "B_wq": _B_in["wq"],
        "B_wo": _B_in["wo"],
    }

def reference(A_x, A_k_cache, A_v_cache, A_wq, A_wo, B_x, B_k_cache, B_v_cache, B_wq, B_wo):
    def _A_ref(x, k_cache, v_cache, wq, wo):
        B, Sq, D = x.shape
        Hq, Dh = 32, 128
        Hkv = k_cache.shape[1]
        rep = Hq // Hkv
        q = (lambda x, *d: x.permute(*d))((x @ wq).reshape(B, Sq, Hq, Dh), 0, 2, 1, 3)
        k = (lambda x, n, axis: torch.repeat_interleave(x, n, dim=axis))(k_cache, rep, axis=1)
        v = (lambda x, n, axis: torch.repeat_interleave(x, n, dim=axis))(v_cache, rep, axis=1)
        scores = torch.einsum("bhqd,bhkd->bhqk", q, k) * (Dh ** -0.5)
        p = _softmax(scores, axis=-1)
        o = torch.einsum("bhqk,bhkd->bhqd", p, v)
        return (lambda x, *d: x.permute(*d))(o, 0, 2, 1, 3).reshape(B, Sq, Hq * Dh) @ wo

    def _B_ref(x, k_cache, v_cache, wq, wo):
        B, Sq, D = x.shape
        Hq, Dh = 32, 128
        Hkv, Sk = k_cache.shape[1], k_cache.shape[2]
        W = min(4096, max(256, Sk // 4))
        rep = Hq // Hkv
        q = (lambda x, *d: x.permute(*d))((x @ wq).reshape(B, Sq, Hq, Dh), 0, 2, 1, 3)
        k = (lambda x, n, axis: torch.repeat_interleave(x, n, dim=axis))(k_cache[:, :, Sk - W:, :], rep, axis=1)
        v = (lambda x, n, axis: torch.repeat_interleave(x, n, dim=axis))(v_cache[:, :, Sk - W:, :], rep, axis=1)
        scores = torch.einsum("bhqd,bhkd->bhqk", q, k) * (Dh ** -0.5)
        p = _softmax(scores, axis=-1)
        o = torch.einsum("bhqk,bhkd->bhqd", p, v)
        return (lambda x, *d: x.permute(*d))(o, 0, 2, 1, 3).reshape(B, Sq, Hq * Dh) @ wo

    a = _A_ref(A_x, A_k_cache, A_v_cache, A_wq, A_wo)
    b = _B_ref(B_x, B_k_cache, B_v_cache, B_wq, B_wo)
    return torch.concatenate([a, b], axis=-1)
```

```cpp
#include <hip/hip_runtime.h>
#include <hip/hip_bf16.h>
#include <stdint.h>


typedef unsigned int u32;
typedef __attribute__((ext_vector_type(4))) unsigned int u32x4;
typedef __attribute__((ext_vector_type(4))) float f32x4;
typedef __attribute__((ext_vector_type(2))) __bf16 bf16x2;

constexpr int NH = 32, SK = 8192, DH = 128, DM = 640, WIN = 2048, HD = NH * DH;
constexpr int NJ = 8;
constexpr int AK = SK / NJ;
constexpr int BK = WIN / NJ;
constexpr int PREC = 132;
constexpr int DEPTH = 12;
constexpr float SCALE = 0.08838834764831845f;
constexpr float LOG2E = 1.4426950408889634f;

__device__ __forceinline__ float dot2(u32 a, u32 b, float c) {
    return __builtin_amdgcn_fdot2_f32_bf16(__builtin_bit_cast(bf16x2, a), __builtin_bit_cast(bf16x2, b), c, false);
}
template <int CTRL> __device__ __forceinline__ float dpp(float x) {
    return __builtin_bit_cast(float, __builtin_amdgcn_mov_dpp(__builtin_bit_cast(int, x), CTRL, 0xf, 0xf, true));
}
constexpr int XOR1 = 0xB1, XOR2 = 0x4E, HMIR = 0x141, MIR = 0x140;
__device__ __forceinline__ float row16_sum(float x) {
    x += dpp<XOR1>(x); x += dpp<XOR2>(x); x += dpp<HMIR>(x); x += dpp<MIR>(x); return x;
}
__device__ __forceinline__ float xrow_sum(float x) {
    auto s = __builtin_amdgcn_permlane16_swap(__float_as_uint(x), __float_as_uint(x), false, false);
    x = __uint_as_float(s[0]) + __uint_as_float(s[1]);
    auto t = __builtin_amdgcn_permlane32_swap(__float_as_uint(x), __float_as_uint(x), false, false);
    return __uint_as_float(t[0]) + __uint_as_float(t[1]);
}
__device__ __forceinline__ float xrow_max(float x) {
    auto s = __builtin_amdgcn_permlane16_swap(__float_as_uint(x), __float_as_uint(x), false, false);
    x = fmaxf(__uint_as_float(s[0]), __uint_as_float(s[1]));
    auto t = __builtin_amdgcn_permlane32_swap(__float_as_uint(x), __float_as_uint(x), false, false);
    return fmaxf(__uint_as_float(t[0]), __uint_as_float(t[1]));
}
__device__ __forceinline__ float bf16r(float x) {
    return (float)(__bf16)x;
}
__device__ __forceinline__ float lo16(u32 w) { return __uint_as_float(w << 16); }
__device__ __forceinline__ float hi16(u32 w) { return __uint_as_float(w & 0xffff0000u); }
__device__ __forceinline__ u32x4 ldnt(const void* p) { return __builtin_nontemporal_load((const u32x4*)p); }
__device__ __forceinline__ u32x4 ld16(const void* p) { return *(const u32x4*)p; }

__global__ void __launch_bounds__(1024) attn_k1(const uint16_t* __restrict__ xA, const uint16_t* __restrict__ kA,
                                                const uint16_t* __restrict__ vA, const uint16_t* __restrict__ wqA,
                                                const uint16_t* __restrict__ xB, const uint16_t* __restrict__ kB,
                                                const uint16_t* __restrict__ vB, const uint16_t* __restrict__ wqB,
                                                float* __restrict__ part) {
    __shared__ __attribute__((aligned(16))) float lds[16 * 2 * 128 + 64 + 128];
    float* qpart = lds;
    float* mls = lds + 16 * 2 * 128;
    u32* qbf = (u32*)(lds + 16 * 2 * 128 + 64);
    const int b = blockIdx.x;
    const int h = (b & 7) + 8 * (b >> 6), j = (b >> 3) & 7;
    const int tid = threadIdx.x, lane = tid & 63, wave = __builtin_amdgcn_readfirstlane(tid >> 6);
    const int r = lane >> 4, c = lane & 15;

    const uint16_t* kap = kA + ((size_t)h * SK + j * AK + wave * 64 + r) * DH + 8 * c;
    const uint16_t* vap = vA + ((size_t)h * SK + j * AK + wave * 64 + r) * DH + 8 * c;
    const uint16_t* kbp = kB + ((size_t)h * SK + (SK - WIN) + j * BK + wave * 16 + r) * DH + 8 * c;
    const uint16_t* vbp = vB + ((size_t)h * SK + (SK - WIN) + j * BK + wave * 16 + r) * DH + 8 * c;

    float qa[8], qb[8];
#pragma unroll
    for (int k = 0; k < 8; ++k) { qa[k] = 0.f; qb[k] = 0.f; }
    {
        const int row0 = wave * 4 + r;
        const uint16_t* wa = wqA + (size_t)row0 * HD + h * DH + 8 * c;
        const uint16_t* wb = wqB + (size_t)row0 * HD + h * DH + 8 * c;
        u32x4 w[10]; float xs[10];
#pragma unroll
        for (int i = 0; i < 10; ++i) { w[i] = ld16(wa + (size_t)i * 64 * HD); xs[i] = __uint_as_float((u32)xA[row0 + i * 64] << 16); }
#pragma unroll
        for (int i = 0; i < 10; ++i)
#pragma unroll
            for (int k = 0; k < 4; ++k) { qa[2 * k] = fmaf(xs[i], lo16(w[i][k]), qa[2 * k]); qa[2 * k + 1] = fmaf(xs[i], hi16(w[i][k]), qa[2 * k + 1]); }
#pragma unroll
        for (int i = 0; i < 10; ++i) { w[i] = ld16(wb + (size_t)i * 64 * HD); xs[i] = __uint_as_float((u32)xB[row0 + i * 64] << 16); }
#pragma unroll
        for (int i = 0; i < 10; ++i)
#pragma unroll
            for (int k = 0; k < 4; ++k) { qb[2 * k] = fmaf(xs[i], lo16(w[i][k]), qb[2 * k]); qb[2 * k + 1] = fmaf(xs[i], hi16(w[i][k]), qb[2 * k + 1]); }
    }
    __builtin_amdgcn_sched_barrier(0);
    u32x4 buf[40];
#define UADDR(n) ((n) < 16 ? kap + (n) * 4 * DH : (n) < 20 ? kbp + ((n) - 16) * 4 * DH : (n) < 36 ? vap + ((n) - 20) * 4 * DH : vbp + ((n) - 36) * 4 * DH)
#pragma unroll
    for (int n = 0; n < DEPTH; ++n) buf[n] = ldnt(UADDR(n));
    __builtin_amdgcn_sched_barrier(0);
#pragma unroll
    for (int k = 0; k < 8; ++k) { qa[k] = xrow_sum(qa[k]); qb[k] = xrow_sum(qb[k]); }
    if (r == 0) {
        *(f32x4*)(qpart + (wave * 2 + 0) * 128 + 8 * c) = f32x4{qa[0], qa[1], qa[2], qa[3]};
        *(f32x4*)(qpart + (wave * 2 + 0) * 128 + 8 * c + 4) = f32x4{qa[4], qa[5], qa[6], qa[7]};
        *(f32x4*)(qpart + (wave * 2 + 1) * 128 + 8 * c) = f32x4{qb[0], qb[1], qb[2], qb[3]};
        *(f32x4*)(qpart + (wave * 2 + 1) * 128 + 8 * c + 4) = f32x4{qb[4], qb[5], qb[6], qb[7]};
    }
    __syncthreads();
    if (tid < 256) {
        float s = 0.f;
#pragma unroll
        for (int w = 0; w < 16; ++w) s += qpart[w * 256 + tid];
        ((uint16_t*)qbf)[tid] = (uint16_t)(__float_as_uint(bf16r(s)) >> 16);
    }
    __syncthreads();
    const u32x4 qA4 = *(const u32x4*)(qbf + 4 * c);
    const u32x4 qB4 = *(const u32x4*)(qbf + 64 + 4 * c);

    float sc[20];
    float mA = -INFINITY, mB = -INFINITY;
    float accA[8], accB[8], lA = 0.f, lB = 0.f;
#pragma unroll
    for (int k = 0; k < 8; ++k) { accA[k] = 0.f; accB[k] = 0.f; }
#pragma unroll
    for (int n = 0; n < 40; ++n) {
        if (n < 20) {
            const u32x4 q4 = n < 16 ? qA4 : qB4;
            float d = dot2(buf[n][0], q4[0], 0.f); d = dot2(buf[n][1], q4[1], d); d = dot2(buf[n][2], q4[2], d); d = dot2(buf[n][3], q4[3], d);
            d = row16_sum(d);
            d = bf16r(bf16r(d) * SCALE) * LOG2E;
            sc[n] = d;
            if (n < 16) mA = fmaxf(mA, d); else mB = fmaxf(mB, d);
            if (n == 19) { mA = xrow_max(mA); mB = xrow_max(mB); }
        } else if (n < 36) {
            const float p = __builtin_amdgcn_exp2f(sc[n - 20] - mA);
            lA += p;
#pragma unroll
            for (int k = 0; k < 4; ++k) { accA[2 * k] = fmaf(p, lo16(buf[n][k]), accA[2 * k]); accA[2 * k + 1] = fmaf(p, hi16(buf[n][k]), accA[2 * k + 1]); }
        } else {
            const float p = __builtin_amdgcn_exp2f(sc[n - 20] - mB);
            lB += p;
#pragma unroll
            for (int k = 0; k < 4; ++k) { accB[2 * k] = fmaf(p, lo16(buf[n][k]), accB[2 * k]); accB[2 * k + 1] = fmaf(p, hi16(buf[n][k]), accB[2 * k + 1]); }
        }
        __builtin_amdgcn_sched_barrier(0);
        if (n + DEPTH < 40) buf[n + DEPTH] = ldnt(UADDR(n + DEPTH));
        __builtin_amdgcn_sched_barrier(0);
    }
#pragma unroll
    for (int k = 0; k < 8; ++k) { accA[k] = xrow_sum(accA[k]); accB[k] = xrow_sum(accB[k]); }
    lA = xrow_sum(lA); lB = xrow_sum(lB);
    if (r == 0) {
        *(f32x4*)(qpart + (wave * 2 + 0) * 128 + 8 * c) = f32x4{accA[0], accA[1], accA[2], accA[3]};
        *(f32x4*)(qpart + (wave * 2 + 0) * 128 + 8 * c + 4) = f32x4{accA[4], accA[5], accA[6], accA[7]};
        *(f32x4*)(qpart + (wave * 2 + 1) * 128 + 8 * c) = f32x4{accB[0], accB[1], accB[2], accB[3]};
        *(f32x4*)(qpart + (wave * 2 + 1) * 128 + 8 * c + 4) = f32x4{accB[4], accB[5], accB[6], accB[7]};
        if (c == 0) { mls[wave * 4 + 0] = mA; mls[wave * 4 + 1] = lA; mls[wave * 4 + 2] = mB; mls[wave * 4 + 3] = lB; }
    }
    __syncthreads();
    if (tid < 256) {
        const int ab = tid >> 7, d = tid & 127;
        float M = -INFINITY;
#pragma unroll
        for (int w = 0; w < 16; ++w) M = fmaxf(M, mls[w * 4 + ab * 2]);
        float L = 0.f, o = 0.f;
#pragma unroll
        for (int w = 0; w < 16; ++w) {
            const float f = __builtin_amdgcn_exp2f(mls[w * 4 + ab * 2] - M);
            L = fmaf(f, mls[w * 4 + ab * 2 + 1], L);
            o = fmaf(f, qpart[w * 256 + tid], o);
        }
        float* rec = part + ((size_t)(ab * NH + h) * NJ + j) * PREC;
        rec[d] = o;
        if (d == 0) { rec[128] = M; rec[129] = L; }
    }
}

constexpr int CB = 16;
__global__ void __launch_bounds__(1024) attn_k2(const float* __restrict__ part, const uint16_t* __restrict__ woA,
                                                const uint16_t* __restrict__ woB, uint16_t* __restrict__ out) {
    __shared__ __attribute__((aligned(16))) float o_s[HD];
    __shared__ float red[16][CB];
    const int ab = blockIdx.x / (DM / CB), cb = blockIdx.x % (DM / CB);
    const int tid = threadIdx.x, lane = tid & 63, wave = tid >> 6;
    const uint16_t* wo = ab ? woB : woA;
    const int rsub = wave * 32 + (lane >> 1), half = lane & 1;
    u32x4 w[8];
#pragma unroll
    for (int i = 0; i < 8; ++i) w[i] = ld16(wo + (size_t)(i * 512 + rsub) * DM + cb * CB + half * 8);
    {
        const int h = tid >> 5, d0 = (tid & 31) * 4;
        const float* rec = part + (size_t)(ab * NH + h) * NJ * PREC;
        float mm[NJ], ll[NJ]; f32x4 a[NJ];
#pragma unroll
        for (int s = 0; s < NJ; ++s) { mm[s] = rec[s * PREC + 128]; ll[s] = rec[s * PREC + 129]; a[s] = *(const f32x4*)(rec + s * PREC + d0); }
        float M = -INFINITY;
#pragma unroll
        for (int s = 0; s < NJ; ++s) M = fmaxf(M, mm[s]);
        float L = 0.f; f32x4 o = {0.f, 0.f, 0.f, 0.f};
#pragma unroll
        for (int s = 0; s < NJ; ++s) { const float f = __builtin_amdgcn_exp2f(mm[s] - M); L = fmaf(f, ll[s], L); o += f * a[s]; }
        const float inv = 1.0f / L;
        *(f32x4*)(o_s + tid * 4) = f32x4{bf16r(o[0] * inv), bf16r(o[1] * inv), bf16r(o[2] * inv), bf16r(o[3] * inv)};
    }
    __syncthreads();
    float acc[8];
#pragma unroll
    for (int k = 0; k < 8; ++k) acc[k] = 0.f;
#pragma unroll
    for (int i = 0; i < 8; ++i) {
        const float ov = o_s[i * 512 + rsub];
#pragma unroll
        for (int k = 0; k < 4; ++k) { acc[2 * k] = fmaf(ov, lo16(w[i][k]), acc[2 * k]); acc[2 * k + 1] = fmaf(ov, hi16(w[i][k]), acc[2 * k + 1]); }
    }
#pragma unroll
    for (int k = 0; k < 8; ++k) {
        float v = acc[k];
        v += __shfl_xor(v, 2); v += __shfl_xor(v, 4); v += __shfl_xor(v, 8); v += __shfl_xor(v, 16); v += __shfl_xor(v, 32);
        acc[k] = v;
    }
    if (lane < 2) {
#pragma unroll
        for (int k = 0; k < 8; ++k) red[wave][half * 8 + k] = acc[k];
    }
    __syncthreads();
    if (tid < CB) {
        float s = 0.f;
#pragma unroll
        for (int w2 = 0; w2 < 16; ++w2) s += red[w2][tid];
        out[ab * DM + cb * CB + tid] = (uint16_t)(__float_as_uint(bf16r(s)) >> 16);
    }
}

extern "C" void kernel_launch(void* const* d_in, const int* in_sizes, int n_in,
                              void* d_out, int out_size, void* d_ws, size_t ws_size,
                              hipStream_t stream) {
    const uint16_t* xA = (const uint16_t*)d_in[0];
    const uint16_t* kA = (const uint16_t*)d_in[1];
    const uint16_t* vA = (const uint16_t*)d_in[2];
    const uint16_t* wqA = (const uint16_t*)d_in[3];
    const uint16_t* woA = (const uint16_t*)d_in[4];
    const uint16_t* xB = (const uint16_t*)d_in[5];
    const uint16_t* kB = (const uint16_t*)d_in[6];
    const uint16_t* vB = (const uint16_t*)d_in[7];
    const uint16_t* wqB = (const uint16_t*)d_in[8];
    const uint16_t* woB = (const uint16_t*)d_in[9];
    float* part = (float*)d_ws;
    attn_k1<<<NH * NJ, 1024, 0, stream>>>(xA, kA, vA, wqA, xB, kB, vB, wqB, part);
    attn_k2<<<2 * (DM / CB), 1024, 0, stream>>>(part, woA, woB, (uint16_t*)d_out);
}
```

```cpp
#include <hip/hip_runtime.h>
#include <hip/hip_bf16.h>
#include <stdint.h>

typedef unsigned int u32;
typedef __attribute__((ext_vector_type(4))) unsigned int u32x4;
typedef __attribute__((ext_vector_type(4))) float f32x4;
typedef __attribute__((ext_vector_type(2))) __bf16 bf16x2;
typedef __amdgpu_buffer_rsrc_t rsrc_t;

constexpr int NH = 32, SK = 8192, DH = 128, DM = 640, WIN = 2048, HD = NH * DH;
constexpr int PREC = 132;
constexpr float SCALE = 0.08838834764831845f;
constexpr float LOG2E = 1.4426950408889634f;
constexpr int QS = 4;
constexpr int KPW = 256;
constexpr int NSA = SK / KPW, NSB = WIN / KPW;
constexpr int DEPTH = 8;
constexpr size_t WS_Q = 0;
constexpr size_t WS_PART = WS_Q + 2 * NH * QS * 128;
constexpr size_t WS_SLAB = WS_PART + (size_t)NH * (NSA + NSB) * PREC;

__device__ __forceinline__ float dot2(u32 a, u32 b, float c) {
    return __builtin_amdgcn_fdot2_f32_bf16(__builtin_bit_cast(bf16x2, a), __builtin_bit_cast(bf16x2, b), c, false);
}
template <int CTRL> __device__ __forceinline__ float dpp(float x) {
    return __builtin_bit_cast(float, __builtin_amdgcn_mov_dpp(__builtin_bit_cast(int, x), CTRL, 0xf, 0xf, true));
}
constexpr int XOR1 = 0xB1, XOR2 = 0x4E, HMIR = 0x141, MIR = 0x140;
__device__ __forceinline__ float row16_sum(float x) {
    x += dpp<XOR1>(x); x += dpp<XOR2>(x); x += dpp<HMIR>(x); x += dpp<MIR>(x); return x;
}
__device__ __forceinline__ float xrow_sum(float x) {
    auto s = __builtin_amdgcn_permlane16_swap(__float_as_uint(x), __float_as_uint(x), false, false);
    x = __uint_as_float(s[0]) + __uint_as_float(s[1]);
    auto t = __builtin_amdgcn_permlane32_swap(__float_as_uint(x), __float_as_uint(x), false, false);
    return __uint_as_float(t[0]) + __uint_as_float(t[1]);
}
__device__ __forceinline__ float xrow_max(float x) {
    auto s = __builtin_amdgcn_permlane16_swap(__float_as_uint(x), __float_as_uint(x), false, false);
    x = fmaxf(__uint_as_float(s[0]), __uint_as_float(s[1]));
    auto t = __builtin_amdgcn_permlane32_swap(__float_as_uint(x), __float_as_uint(x), false, false);
    return fmaxf(__uint_as_float(t[0]), __uint_as_float(t[1]));
}
__device__ __forceinline__ float bf16r(float x) { return (float)(__bf16)x; }
__device__ __forceinline__ float lo16(u32 w) { return __uint_as_float(w << 16); }
__device__ __forceinline__ float hi16(u32 w) { return __uint_as_float(w & 0xffff0000u); }
__device__ __forceinline__ u32 pack2(float a, float b) { return (__float_as_uint(bf16r(a)) >> 16) | (__float_as_uint(bf16r(b)) & 0xffff0000u); }
__device__ __forceinline__ rsrc_t mkrsrc(const void* p, u32 bytes) { return __builtin_amdgcn_make_buffer_rsrc((void*)p, 0, bytes, 0x00020000); }
template <int AUX> __device__ __forceinline__ u32x4 bld(rsrc_t r, u32 off) { return __builtin_amdgcn_raw_buffer_load_b128(r, off, 0, AUX); }

__global__ void __launch_bounds__(256) k0_qproj(const uint16_t* __restrict__ xA, const uint16_t* __restrict__ wqA,
                                               const uint16_t* __restrict__ xB, const uint16_t* __restrict__ wqB, float* __restrict__ ws) {
    __shared__ __attribute__((aligned(16))) float red[4][128];
    const int b = blockIdx.x, ab = b / (NH * QS), h = (b / QS) % NH, qs = b % QS;
    const int tid = threadIdx.x, lane = tid & 63, wave = tid >> 6, r = lane >> 4, c = lane & 15;
    const uint16_t* x = ab ? xB : xA;
    const rsrc_t rw = mkrsrc(ab ? wqB : wqA, DM * HD * 2);
    constexpr int RPW = DM / QS;
    constexpr int NP = RPW / 16;
    const int row0 = qs * RPW + wave * 4 + r;
    u32x4 w[NP]; float xs[NP];
#pragma unroll
    for (int i = 0; i < NP; ++i) { w[i] = bld<2>(rw, ((row0 + i * 16) * HD + h * DH + 8 * c) * 2); xs[i] = __uint_as_float((u32)x[row0 + i * 16] << 16); }
    float q[8];
#pragma unroll
    for (int k = 0; k < 8; ++k) q[k] = 0.f;
#pragma unroll
    for (int i = 0; i < NP; ++i)
#pragma unroll
        for (int k = 0; k < 4; ++k) { q[2 * k] = fmaf(xs[i], lo16(w[i][k]), q[2 * k]); q[2 * k + 1] = fmaf(xs[i], hi16(w[i][k]), q[2 * k + 1]); }
#pragma unroll
    for (int k = 0; k < 8; ++k) q[k] = xrow_sum(q[k]);
    if (r == 0) { *(f32x4*)&red[wave][8 * c] = f32x4{q[0], q[1], q[2], q[3]}; *(f32x4*)&red[wave][8 * c + 4] = f32x4{q[4], q[5], q[6], q[7]}; }
    __syncthreads();
    if (tid < 128) ws[WS_Q + (size_t)b * 128 + tid] = (red[0][tid] + red[1][tid]) + (red[2][tid] + red[3][tid]);
}

__global__ void __launch_bounds__(256) k1_attn(const uint16_t* __restrict__ kA, const uint16_t* __restrict__ vA,
                                              const uint16_t* __restrict__ kB, const uint16_t* __restrict__ vB, float* __restrict__ ws) {
    __shared__ __attribute__((aligned(16))) float accs[4][128];
    __shared__ float mls[4][2];
    const int b = blockIdx.x;
    const bool isA = b < NH * NSA;
    const int h = isA ? b / NSA : (b - NH * NSA) / NSB;
    const int key0 = isA ? (b % NSA) * KPW : (SK - WIN) + ((b - NH * NSA) % NSB) * KPW;
    const int tid = threadIdx.x, lane = tid & 63, wave = __builtin_amdgcn_readfirstlane(tid >> 6), r = lane >> 4, c = lane & 15;
    const rsrc_t rk = mkrsrc(isA ? kA : kB, NH * SK * DH * 2), rv = mkrsrc(isA ? vA : vB, NH * SK * DH * 2);
    const u32 off = ((h * SK + key0 + wave * 64 + r) * DH + 8 * c) * 2;
    const float* qp = ws + WS_Q + (size_t)((isA ? 0 : NH) + h) * QS * 128 + 8 * c;
    f32x4 q0[QS], q1[QS];
#pragma unroll
    for (int s = 0; s < QS; ++s) { q0[s] = *(const f32x4*)(qp + s * 128); q1[s] = *(const f32x4*)(qp + s * 128 + 4); }
    u32x4 buf[32];
#define ULD(n) ((n) < 16 ? bld<2>(rk, off + (n) * 1024) : bld<2>(rv, off + ((n) - 16) * 1024))
#pragma unroll
    for (int n = 0; n < DEPTH; ++n) buf[n] = ULD(n);
    f32x4 qa = (q0[0] + q0[1]) + (q0[2] + q0[3]), qb = (q1[0] + q1[1]) + (q1[2] + q1[3]);
    const u32x4 q4 = {pack2(qa[0], qa[1]), pack2(qa[2], qa[3]), pack2(qb[0], qb[1]), pack2(qb[2], qb[3])};
    float sc[16], m = -INFINITY, l = 0.f, acc[8];
#pragma unroll
    for (int k = 0; k < 8; ++k) acc[k] = 0.f;
#pragma unroll
    for (int n = 0; n < 32; ++n) {
        if (n < 16) {
            float d = dot2(buf[n][0], q4[0], 0.f); d = dot2(buf[n][1], q4[1], d); d = dot2(buf[n][2], q4[2], d); d = dot2(buf[n][3], q4[3], d);
            d = row16_sum(d);
            d = bf16r(bf16r(d) * SCALE) * LOG2E;
            sc[n] = d; m = fmaxf(m, d);
            if (n == 15) m = xrow_max(m);
        } else {
            const float p = __builtin_amdgcn_exp2f(sc[n - 16] - m);
            l += p;
#pragma unroll
            for (int k = 0; k < 4; ++k) { acc[2 * k] = fmaf(p, lo16(buf[n][k]), acc[2 * k]); acc[2 * k + 1] = fmaf(p, hi16(buf[n][k]), acc[2 * k + 1]); }
        }
        __builtin_amdgcn_sched_barrier(0);
        if (n + DEPTH < 32) buf[n + DEPTH] = ULD(n + DEPTH);
        __builtin_amdgcn_sched_barrier(0);
    }
#pragma unroll
    for (int k = 0; k < 8; ++k) acc[k] = xrow_sum(acc[k]);
    l = xrow_sum(l);
    if (r == 0) {
        *(f32x4*)&accs[wave][8 * c] = f32x4{acc[0], acc[1], acc[2], acc[3]};
        *(f32x4*)&accs[wave][8 * c + 4] = f32x4{acc[4], acc[5], acc[6], acc[7]};
        if (c == 0) { mls[wave][0] = m; mls[wave][1] = l; }
    }
    __syncthreads();
    if (tid < 128) {
        const float M = fmaxf(fmaxf(mls[0][0], mls[1][0]), fmaxf(mls[2][0], mls[3][0]));
        float L = 0.f, o = 0.f;
#pragma unroll
        for (int w = 0; w < 4; ++w) {
            const float f = __builtin_amdgcn_exp2f(mls[w][0] - M);
            L = fmaf(f, mls[w][1], L); o = fmaf(f, accs[w][tid], o);
        }
        float* rec = ws + WS_PART + (size_t)b * PREC;
        rec[tid] = o;
        if (tid == 0) { rec[128] = M; rec[129] = L; }
    }
}

__global__ void __launch_bounds__(640) k2_out(const uint16_t* __restrict__ woA, const uint16_t* __restrict__ woB, float* __restrict__ ws) {
    __shared__ float o_s[128];
    __shared__ float wts[32];
    __shared__ __attribute__((aligned(16))) float red[8][640];
    const int ab = blockIdx.x / NH, h = blockIdx.x % NH;
    const int tid = threadIdx.x;
    const rsrc_t rw = mkrsrc(ab ? woB : woA, HD * DM * 2);
    const int chunk = tid % 80, rg = tid / 80;
    u32x4 w[16];
#pragma unroll
    for (int i = 0; i < 16; ++i) w[i] = bld<2>(rw, ((h * DH + rg + 8 * i) * DM + chunk * 8) * 2);
    const int ns = ab ? NSB : NSA;
    const float* rec = ws + WS_PART + (ab ? (size_t)NH * NSA * PREC + (size_t)h * NSB * PREC : (size_t)h * NSA * PREC);
    if (tid < 64) {
        const float mm = tid < ns ? rec[tid * PREC + 128] : -INFINITY, ll = tid < ns ? rec[tid * PREC + 129] : 0.f;
        float M = mm;
#pragma unroll
        for (int o = 32; o >= 1; o >>= 1) M = fmaxf(M, __shfl_xor(M, o));
        const float f = __builtin_amdgcn_exp2f(mm - M);
        float L = f * ll;
#pragma unroll
        for (int o = 32; o >= 1; o >>= 1) L += __shfl_xor(L, o);
        if (tid < 32) wts[tid] = f / L;
    }
    __syncthreads();
    if (tid < 128) {
        float o = 0.f;
        for (int s = 0; s < ns; ++s) o = fmaf(wts[s], rec[s * PREC + tid], o);
        o_s[tid] = bf16r(o);
    }
    __syncthreads();
    float acc[8];
#pragma unroll
    for (int k = 0; k < 8; ++k) acc[k] = 0.f;
#pragma unroll
    for (int i = 0; i < 16; ++i) {
        const float ov = o_s[rg + 8 * i];
#pragma unroll
        for (int k = 0; k < 4; ++k) { acc[2 * k] = fmaf(ov, lo16(w[i][k]), acc[2 * k]); acc[2 * k + 1] = fmaf(ov, hi16(w[i][k]), acc[2 * k + 1]); }
    }
    *(f32x4*)&red[rg][chunk * 8] = f32x4{acc[0], acc[1], acc[2], acc[3]};
    *(f32x4*)&red[rg][chunk * 8 + 4] = f32x4{acc[4], acc[5], acc[6], acc[7]};
    __syncthreads();
    float s = 0.f;
#pragma unroll
    for (int g = 0; g < 8; ++g) s += red[g][tid];
    ws[WS_SLAB + (size_t)blockIdx.x * DM + tid] = s;
}

__global__ void __launch_bounds__(256) k3_red(const float* __restrict__ ws, uint16_t* __restrict__ out) {
    __shared__ float red[4][64];
    const int col = blockIdx.x * 64 + (threadIdx.x & 63), g = threadIdx.x >> 6;
    const int ab = col / DM, cc = col % DM;
    float v[8];
#pragma unroll
    for (int i = 0; i < 8; ++i) v[i] = ws[WS_SLAB + (size_t)(ab * NH + g * 8 + i) * DM + cc];
    float s = 0.f;
#pragma unroll
    for (int i = 0; i < 8; ++i) s += v[i];
    red[g][threadIdx.x & 63] = s;
    __syncthreads();
    if (g == 0) { const float t = (red[0][threadIdx.x] + red[1][threadIdx.x]) + (red[2][threadIdx.x] + red[3][threadIdx.x]); out[col] = (uint16_t)(__float_as_uint(bf16r(t)) >> 16); }
}

extern "C" void kernel_launch(void* const* d_in, const int* in_sizes, int n_in,
                              void* d_out, int out_size, void* d_ws, size_t ws_size,
                              hipStream_t stream) {
    const uint16_t* xA = (const uint16_t*)d_in[0];
    const uint16_t* kA = (const uint16_t*)d_in[1];
    const uint16_t* vA = (const uint16_t*)d_in[2];
    const uint16_t* wqA = (const uint16_t*)d_in[3];
    const uint16_t* woA = (const uint16_t*)d_in[4];
    const uint16_t* xB = (const uint16_t*)d_in[5];
    const uint16_t* kB = (const uint16_t*)d_in[6];
    const uint16_t* vB = (const uint16_t*)d_in[7];
    const uint16_t* wqB = (const uint16_t*)d_in[8];
    const uint16_t* woB = (const uint16_t*)d_in[9];
    float* ws = (float*)d_ws;
    k0_qproj<<<2 * NH * QS, 256, 0, stream>>>(xA, wqA, xB, wqB, ws);
    k1_attn<<<NH * (NSA + NSB), 256, 0, stream>>>(kA, vA, kB, vB, ws);
    k2_out<<<2 * NH, 640, 0, stream>>>(woA, woB, ws);
    k3_red<<<20, 256, 0, stream>>>(ws, (uint16_t*)d_out);
}
```

```cpp
#include <hip/hip_runtime.h>
#include <hip/hip_bf16.h>
#include <stdint.h>

typedef unsigned int u32;
typedef unsigned long long u64;
typedef __attribute__((ext_vector_type(4))) unsigned int u32x4;
typedef __attribute__((ext_vector_type(2))) unsigned int u32x2;
typedef __attribute__((ext_vector_type(4))) float f32x4;
typedef __attribute__((ext_vector_type(2))) __bf16 bf16x2;
typedef __amdgpu_buffer_rsrc_t rsrc_t;

constexpr int NH = 32, SK = 8192, DH = 128, DM = 640, WIN = 2048, HD = NH * DH;
constexpr float SCALE = 0.08838834764831845f;
constexpr float LOG2E = 1.4426950408889634f;
constexpr int KPW = 256;
constexpr int NSA = SK / KPW, NSB = WIN / KPW;
constexpr int NATT = NH * (NSA + NSB);
constexpr int NC5 = 5;
constexpr int NHELP = 2 * NH * NC5;
constexpr int NWG = NHELP + NATT;
constexpr int QS = 4;
#define DEPTH 6
#define POLL_SLEEP 8
constexpr u32 SPIN_MAX = 1u << 20;
constexpr u32 CNT_OFF = 0;
constexpr int NCNT = 2 * NH + 2 * NC5;
constexpr u32 Q_OFF = 16384;
constexpr u32 PSTR = 640;
constexpr u32 P_OFF = Q_OFF + 2 * NH * QS * 512;
constexpr u32 Y_OFF = P_OFF + NATT * PSTR;
constexpr u32 WS_BYTES = Y_OFF + NHELP * 512;

__device__ __forceinline__ float dot2(u32 a, u32 b, float c) {
    return __builtin_amdgcn_fdot2_f32_bf16(__builtin_bit_cast(bf16x2, a), __builtin_bit_cast(bf16x2, b), c, false);
}
template <int CTRL> __device__ __forceinline__ float dpp(float x) {
    return __builtin_bit_cast(float, __builtin_amdgcn_mov_dpp(__builtin_bit_cast(int, x), CTRL, 0xf, 0xf, true));
}
constexpr int XOR1 = 0xB1, XOR2 = 0x4E, HMIR = 0x141, MIR = 0x140;
__device__ __forceinline__ float row16_sum(float x) {
    x += dpp<XOR1>(x); x += dpp<XOR2>(x); x += dpp<HMIR>(x); x += dpp<MIR>(x); return x;
}
__device__ __forceinline__ float xrow_sum(float x) {
    auto s = __builtin_amdgcn_permlane16_swap(__float_as_uint(x), __float_as_uint(x), false, false);
    x = __uint_as_float(s[0]) + __uint_as_float(s[1]);
    auto t = __builtin_amdgcn_permlane32_swap(__float_as_uint(x), __float_as_uint(x), false, false);
    return __uint_as_float(t[0]) + __uint_as_float(t[1]);
}
__device__ __forceinline__ float xhalf_sum(float x) {
    auto t = __builtin_amdgcn_permlane32_swap(__float_as_uint(x), __float_as_uint(x), false, false);
    return __uint_as_float(t[0]) + __uint_as_float(t[1]);
}
__device__ __forceinline__ float xrow_max(float x) {
    auto s = __builtin_amdgcn_permlane16_swap(__float_as_uint(x), __float_as_uint(x), false, false);
    x = fmaxf(__uint_as_float(s[0]), __uint_as_float(s[1]));
    auto t = __builtin_amdgcn_permlane32_swap(__float_as_uint(x), __float_as_uint(x), false, false);
    return fmaxf(__uint_as_float(t[0]), __uint_as_float(t[1]));
}
__device__ __forceinline__ float bf16r(float x) { return (float)(__bf16)x; }
__device__ __forceinline__ float lo16(u32 w) { return __uint_as_float(w << 16); }
__device__ __forceinline__ float hi16(u32 w) { return __uint_as_float(w & 0xffff0000u); }
__device__ __forceinline__ u32 pack2(float a, float b) { return (__float_as_uint(bf16r(a)) >> 16) | (__float_as_uint(bf16r(b)) & 0xffff0000u); }
__device__ __forceinline__ rsrc_t mkrsrc(const void* p, u32 bytes) { return __builtin_amdgcn_make_buffer_rsrc((void*)p, 0, bytes, 0x00020000); }
template <int AUX> __device__ __forceinline__ u32x4 bld(rsrc_t r, u32 off, u32 soff = 0) { return __builtin_amdgcn_raw_buffer_load_b128(r, off, soff, AUX); }
__device__ __forceinline__ f32x4 ld_sc1(rsrc_t r, u32 off) { return __builtin_bit_cast(f32x4, __builtin_amdgcn_raw_buffer_load_b128(r, off, 0, 16)); }
__device__ __forceinline__ void st_sc1(rsrc_t r, u32 off, f32x4 v) { __builtin_amdgcn_raw_buffer_store_b128(__builtin_bit_cast(u32x4, v), r, off, 0, 16); }
__device__ __forceinline__ void wait_cnt(const u32* cnt, u32 want) {
#pragma nounroll
    for (u32 spin = 0; spin < SPIN_MAX; ++spin) {
        const u32 v = __hip_atomic_load(cnt, __ATOMIC_RELAXED, __HIP_MEMORY_SCOPE_AGENT);
        if ((u32)__builtin_amdgcn_readfirstlane(v) >= want) break;
        __builtin_amdgcn_s_sleep(POLL_SLEEP);
    }
    asm volatile("" ::: "memory");
}

__global__ void __launch_bounds__(256) k0_qproj(const uint16_t* __restrict__ xA, const uint16_t* __restrict__ wqA,
                                               const uint16_t* __restrict__ xB, const uint16_t* __restrict__ wqB, unsigned char* __restrict__ ws) {
    __shared__ __attribute__((aligned(16))) float red[4][128];
    const int b = blockIdx.x, ab = b / (NH * QS), h = (b / QS) % NH, qs = b % QS;
    const int tid = threadIdx.x, lane = tid & 63, wave = tid >> 6, r = lane >> 4, c = lane & 15;
    const uint16_t* x = ab ? xB : xA;
    const rsrc_t rw = mkrsrc(ab ? wqB : wqA, DM * HD * 2);
    constexpr int RPW = DM / QS, NP = RPW / 16;
    const int row0 = qs * RPW + wave * 4 + r;
    u32x4 w[NP]; float xs[NP];
#pragma unroll
    for (int i = 0; i < NP; ++i) { w[i] = bld<2>(rw, ((row0 + i * 16) * HD + h * DH + 8 * c) * 2); xs[i] = __uint_as_float((u32)x[row0 + i * 16] << 16); }
    if (b == 0 && tid < NCNT) *(u32*)(ws + CNT_OFF + tid * 128) = 0u;
    float q[8];
#pragma unroll
    for (int k = 0; k < 8; ++k) q[k] = 0.f;
#pragma unroll
    for (int i = 0; i < NP; ++i)
#pragma unroll
        for (int k = 0; k < 4; ++k) { q[2 * k] = fmaf(xs[i], lo16(w[i][k]), q[2 * k]); q[2 * k + 1] = fmaf(xs[i], hi16(w[i][k]), q[2 * k + 1]); }
#pragma unroll
    for (int k = 0; k < 8; ++k) q[k] = xrow_sum(q[k]);
    if (r == 0) { *(f32x4*)&red[wave][8 * c] = f32x4{q[0], q[1], q[2], q[3]}; *(f32x4*)&red[wave][8 * c + 4] = f32x4{q[4], q[5], q[6], q[7]}; }
    __syncthreads();
    if (tid < 128) ((float*)(ws + Q_OFF))[(size_t)b * 128 + tid] = (red[0][tid] + red[1][tid]) + (red[2][tid] + red[3][tid]);
}

__global__ void __launch_bounds__(256, 7) fused(const uint16_t* __restrict__ kA, const uint16_t* __restrict__ vA, const uint16_t* __restrict__ woA,
                                                const uint16_t* __restrict__ kB, const uint16_t* __restrict__ vB, const uint16_t* __restrict__ woB,
                                                unsigned char* __restrict__ ws, uint16_t* __restrict__ out) {
    __shared__ __attribute__((aligned(16))) float red[8][128];
    __shared__ __attribute__((aligned(16))) float vec[128];
    __shared__ float mls[4][2];
    const int b = blockIdx.x;
    const int tid = threadIdx.x, lane = tid & 63, wave = __builtin_amdgcn_readfirstlane(tid >> 6), r = lane >> 4, c = lane & 15;
    const rsrc_t rws = mkrsrc(ws, WS_BYTES);

    if (b >= NHELP) {
        const int a = b - NHELP;
        const bool isA = a < NH * NSA;
        const int h = isA ? a / NSA : (a - NH * NSA) / NSB;
        const int key0 = isA ? (a % NSA) * KPW : (SK - WIN) + ((a - NH * NSA) % NSB) * KPW;
        const rsrc_t rk = mkrsrc(isA ? kA : kB, NH * SK * DH * 2), rv = mkrsrc(isA ? vA : vB, NH * SK * DH * 2);
        const u32 off = ((h * SK + key0 + wave * 64 + r) * DH + 8 * c) * 2;
        u32x4 q4;
        const float* qp = (const float*)(ws + Q_OFF) + (size_t)((isA ? 0 : NH) + h) * QS * 128 + 8 * c;
        f32x4 q0[QS], q1[QS];
#pragma unroll
        for (int s2 = 0; s2 < QS; ++s2) { q0[s2] = *(const f32x4*)(qp + s2 * 128); q1[s2] = *(const f32x4*)(qp + s2 * 128 + 4); }
        u32x4 buf[32];
#define ULD(n) (((n) & 8) ? bld<2>(rv, off, (((n) >> 4) * 8 + ((n) & 7)) * 1024) : bld<2>(rk, off, (((n) >> 4) * 8 + ((n) & 7)) * 1024))
#pragma unroll
        for (int n = 0; n < DEPTH; ++n) buf[n] = ULD(n);
        {
            const f32x4 qa = (q0[0] + q0[1]) + (q0[2] + q0[3]), qb = (q1[0] + q1[1]) + (q1[2] + q1[3]);
            q4 = u32x4{pack2(qa[0], qa[1]), pack2(qa[2], qa[3]), pack2(qb[0], qb[1]), pack2(qb[2], qb[3])};
        }
        float sc[8], m = -INFINITY, mc = -INFINITY, l = 0.f, acc[8];
#pragma unroll
        for (int k = 0; k < 8; ++k) acc[k] = 0.f;
#pragma unroll
        for (int n = 0; n < 32; ++n) {
            if ((n & 8) == 0) {
                float d = dot2(buf[n][0], q4[0], 0.f); d = dot2(buf[n][1], q4[1], d); d = dot2(buf[n][2], q4[2], d); d = dot2(buf[n][3], q4[3], d);
                d = row16_sum(d);
                d = bf16r(bf16r(d) * SCALE) * LOG2E;
                sc[n & 7] = d; mc = fmaxf(mc, d);
                if ((n & 7) == 7) {
                    mc = xrow_max(mc);
                    if (n > 8) {
                        const float mn = fmaxf(m, mc), f = __builtin_amdgcn_exp2f(m - mn);
                        l *= f;
#pragma unroll
                        for (int k = 0; k < 8; ++k) acc[k] *= f;
                        m = mn;
                    } else m = mc;
                }
            } else {
                const float p = __builtin_amdgcn_exp2f(sc[n & 7] - m);
                l += p;
#pragma unroll
                for (int k = 0; k < 4; ++k) { acc[2 * k] = fmaf(p, lo16(buf[n][k]), acc[2 * k]); acc[2 * k + 1] = fmaf(p, hi16(buf[n][k]), acc[2 * k + 1]); }
            }
            __builtin_amdgcn_sched_barrier(0);
            if (n + DEPTH < 32) buf[n + DEPTH] = ULD(n + DEPTH);
            __builtin_amdgcn_sched_barrier(0);
        }
#pragma unroll
        for (int k = 0; k < 8; ++k) acc[k] = xrow_sum(acc[k]);
        l = xrow_sum(l);
        if (r == 0) {
            *(f32x4*)&red[wave][8 * c] = f32x4{acc[0], acc[1], acc[2], acc[3]};
            *(f32x4*)&red[wave][8 * c + 4] = f32x4{acc[4], acc[5], acc[6], acc[7]};
            if (c == 0) { mls[wave][0] = m; mls[wave][1] = l; }
        }
        __syncthreads();
        if (wave == 0) {
            const float M = fmaxf(fmaxf(mls[0][0], mls[1][0]), fmaxf(mls[2][0], mls[3][0]));
            f32x4 o = {0.f, 0.f, 0.f, 0.f}; float L = 0.f;
#pragma unroll
            for (int w = 0; w < 4; ++w) {
                const float f = __builtin_amdgcn_exp2f(mls[w][0] - M);
                o += f * *(const f32x4*)&red[w][4 * (lane & 31)];
                L = fmaf(f, mls[w][1], L);
            }
            if (lane == 32) o = f32x4{M, L, 0.f, 0.f};
            if (lane <= 32) st_sc1(rws, P_OFF + a * PSTR + lane * 16, o);
            asm volatile("s_waitcnt vmcnt(0)" ::: "memory");
            if (lane == 0) __hip_atomic_fetch_add((u32*)(ws + CNT_OFF + ((isA ? 0 : NH) + h) * 128), 1u, __ATOMIC_RELAXED, __HIP_MEMORY_SCOPE_AGENT);
        }
        return;
    }
    const int ab = b / (NH * NC5), h = (b / NC5) % NH, c5 = b % NC5;
    const int rg = tid >> 4;
    u32x4 wo[8];
    {
        const rsrc_t ro = mkrsrc(ab ? woB : woA, HD * DM * 2);
#pragma unroll
        for (int i = 0; i < 8; ++i) wo[i] = bld<2>(ro, ((h * DH + rg + 16 * i) * DM + c5 * 128 + 8 * c) * 2);
    }
    const int ns = ab ? NSB : NSA;
    const u32 pbase = P_OFF + (ab ? NH * NSA + h * NSB : h * NSA) * PSTR;
    if (wave == 0) wait_cnt((const u32*)(ws + CNT_OFF + (ab * NH + h) * 128), ns);
    __syncthreads();
    {
        const int half = lane >> 5, l32 = lane & 31, s0 = wave * 2 + half;
        f32x4 g[4];
        const f32x4 hd = ld_sc1(rws, pbase + (lane < ns ? lane : 0) * PSTR + 512);
        if (ab == 0) {
#pragma unroll
            for (int i = 0; i < 4; ++i) g[i] = ld_sc1(rws, pbase + (s0 + 8 * i) * PSTR + l32 * 16);
        } else g[0] = ld_sc1(rws, pbase + s0 * PSTR + l32 * 16);
        const float mm = lane < ns ? hd[0] : -INFINITY, ll = lane < ns ? hd[1] : 0.f;
        float M = mm;
#pragma unroll
        for (int o = 32; o >= 1; o >>= 1) M = fmaxf(M, __shfl_xor(M, o));
        const float f = __builtin_amdgcn_exp2f(mm - M);
        float L = f * ll;
#pragma unroll
        for (int o = 32; o >= 1; o >>= 1) L += __shfl_xor(L, o);
        const float wt = f / L;
        f32x4 o = __shfl(wt, s0) * g[0];
        if (ab == 0) {
#pragma unroll
            for (int i = 1; i < 4; ++i) o += __shfl(wt, s0 + 8 * i) * g[i];
        }
        *(f32x4*)&red[s0][4 * l32] = o;
    }
    __syncthreads();
    if (tid < 128) vec[tid] = bf16r(((red[0][tid] + red[1][tid]) + (red[2][tid] + red[3][tid])) + ((red[4][tid] + red[5][tid]) + (red[6][tid] + red[7][tid])));
    __syncthreads();
    {
        float acc[8];
#pragma unroll
        for (int k = 0; k < 8; ++k) acc[k] = 0.f;
#pragma unroll
        for (int i = 0; i < 8; ++i) {
            const float ov = vec[rg + 16 * i];
#pragma unroll
            for (int k = 0; k < 4; ++k) { acc[2 * k] = fmaf(ov, lo16(wo[i][k]), acc[2 * k]); acc[2 * k + 1] = fmaf(ov, hi16(wo[i][k]), acc[2 * k + 1]); }
        }
#pragma unroll
        for (int k = 0; k < 8; ++k) acc[k] = xrow_sum(acc[k]);
        if (r == 0) { *(f32x4*)&red[wave][8 * c] = f32x4{acc[0], acc[1], acc[2], acc[3]}; *(f32x4*)&red[wave][8 * c + 4] = f32x4{acc[4], acc[5], acc[6], acc[7]}; }
    }
    __syncthreads();
    const u32 ybase = Y_OFF + (ab * NC5 + c5) * NH * 512;
    u32* fcnt = (u32*)(ws + CNT_OFF + (2 * NH + ab * NC5 + c5) * 128);
    if (wave == 0) {
        if (lane < 32) {
            const f32x4 y = (*(const f32x4*)&red[0][4 * lane] + *(const f32x4*)&red[1][4 * lane]) + (*(const f32x4*)&red[2][4 * lane] + *(const f32x4*)&red[3][4 * lane]);
            st_sc1(rws, ybase + h * 512 + lane * 16, y);
        }
        asm volatile("s_waitcnt vmcnt(0)" ::: "memory");
        if (lane == 0) __hip_atomic_fetch_add(fcnt, 1u, __ATOMIC_RELAXED, __HIP_MEMORY_SCOPE_AGENT);
    }
    if (h != ab * NC5 + c5) return;
    if (wave == 0) wait_cnt(fcnt, NH);
    __syncthreads();
    {
        const int half = lane >> 5, l32 = lane & 31, s0 = wave * 2 + half;
        f32x4 g[4];
#pragma unroll
        for (int i = 0; i < 4; ++i) g[i] = ld_sc1(rws, ybase + (s0 + 8 * i) * 512 + l32 * 16);
        *(f32x4*)&red[s0][4 * l32] = (g[0] + g[1]) + (g[2] + g[3]);
    }
    __syncthreads();
    if (tid < 128) out[ab * DM + c5 * 128 + tid] = (uint16_t)(__float_as_uint(bf16r(((red[0][tid] + red[1][tid]) + (red[2][tid] + red[3][tid])) + ((red[4][tid] + red[5][tid]) + (red[6][tid] + red[7][tid])))) >> 16);
}

extern "C" void kernel_launch(void* const* d_in, const int* in_sizes, int n_in,
                              void* d_out, int out_size, void* d_ws, size_t ws_size,
                              hipStream_t stream) {
    k0_qproj<<<2 * NH * QS, 256, 0, stream>>>((const uint16_t*)d_in[0], (const uint16_t*)d_in[3], (const uint16_t*)d_in[5], (const uint16_t*)d_in[8], (unsigned char*)d_ws);
    fused<<<NWG, 256, 0, stream>>>((const uint16_t*)d_in[1], (const uint16_t*)d_in[2], (const uint16_t*)d_in[4],
                                   (const uint16_t*)d_in[6], (const uint16_t*)d_in[7], (const uint16_t*)d_in[9],
                                   (unsigned char*)d_ws, (uint16_t*)d_out);
}
```

```cpp
#include <hip/hip_runtime.h>
#include <hip/hip_bf16.h>
#include <stdint.h>

typedef unsigned int u32;
typedef unsigned long long u64;
typedef __attribute__((ext_vector_type(4))) unsigned int u32x4;
typedef __attribute__((ext_vector_type(2))) unsigned int u32x2;
typedef __attribute__((ext_vector_type(4))) float f32x4;
typedef __attribute__((ext_vector_type(2))) __bf16 bf16x2;
typedef __amdgpu_buffer_rsrc_t rsrc_t;

constexpr int NH = 32, SK = 8192, DH = 128, DM = 640, WIN = 2048, HD = NH * DH;
constexpr float SCALE = 0.08838834764831845f;
constexpr float LOG2E = 1.4426950408889634f;
constexpr int KPW = 256;
constexpr int NSA = SK / KPW, NSB = WIN / KPW;
constexpr int NATT = NH * (NSA + NSB);
constexpr int NC5 = 5;
constexpr int NHELP = 2 * NH * NC5;
constexpr int NWG = NHELP + NATT;
constexpr int QS = 4;
#define DEPTH 6
#define POLL_SLEEP 8
constexpr u32 SPIN_MAX = 1u << 20;
constexpr u32 CNT_OFF = 0;
constexpr int NCNT = 2 * NH + 2 * NC5;
constexpr u32 Q_OFF = 16384;
constexpr u32 PSTR = 640;
constexpr u32 P_OFF = Q_OFF + 2 * NH * QS * 512;
constexpr u32 Y_OFF = P_OFF + NATT * PSTR;
constexpr u32 WS_BYTES = Y_OFF + NHELP * 512;

__device__ __forceinline__ float dot2(u32 a, u32 b, float c) {
    return __builtin_amdgcn_fdot2_f32_bf16(__builtin_bit_cast(bf16x2, a), __builtin_bit_cast(bf16x2, b), c, false);
}
template <int CTRL> __device__ __forceinline__ float dpp(float x) {
    return __builtin_bit_cast(float, __builtin_amdgcn_mov_dpp(__builtin_bit_cast(int, x), CTRL, 0xf, 0xf, true));
}
constexpr int XOR1 = 0xB1, XOR2 = 0x4E, HMIR = 0x141, MIR = 0x140;
__device__ __forceinline__ float row16_sum(float x) {
    x += dpp<XOR1>(x); x += dpp<XOR2>(x); x += dpp<HMIR>(x); x += dpp<MIR>(x); return x;
}
__device__ __forceinline__ float xrow_sum(float x) {
    auto s = __builtin_amdgcn_permlane16_swap(__float_as_uint(x), __float_as_uint(x), false, false);
    x = __uint_as_float(s[0]) + __uint_as_float(s[1]);
    auto t = __builtin_amdgcn_permlane32_swap(__float_as_uint(x), __float_as_uint(x), false, false);
    return __uint_as_float(t[0]) + __uint_as_float(t[1]);
}
__device__ __forceinline__ float xhalf_sum(float x) {
    auto t = __builtin_amdgcn_permlane32_swap(__float_as_uint(x), __float_as_uint(x), false, false);
    return __uint_as_float(t[0]) + __uint_as_float(t[1]);
}
__device__ __forceinline__ float xrow_max(float x) {
    auto s = __builtin_amdgcn_permlane16_swap(__float_as_uint(x), __float_as_uint(x), false, false);
    x = fmaxf(__uint_as_float(s[0]), __uint_as_float(s[1]));
    auto t = __builtin_amdgcn_permlane32_swap(__float_as_uint(x), __float_as_uint(x), false, false);
    return fmaxf(__uint_as_float(t[0]), __uint_as_float(t[1]));
}
__device__ __forceinline__ float bf16r(float x) { return (float)(__bf16)x; }
__device__ __forceinline__ float lo16(u32 w) { return __uint_as_float(w << 16); }
__device__ __forceinline__ float hi16(u32 w) { return __uint_as_float(w & 0xffff0000u); }
__device__ __forceinline__ u32 pack2(float a, float b) { return (__float_as_uint(bf16r(a)) >> 16) | (__float_as_uint(bf16r(b)) & 0xffff0000u); }
__device__ __forceinline__ rsrc_t mkrsrc(const void* p, u32 bytes) { return __builtin_amdgcn_make_buffer_rsrc((void*)p, 0, bytes, 0x00020000); }
template <int AUX> __device__ __forceinline__ u32x4 bld(rsrc_t r, u32 off, u32 soff = 0) { return __builtin_amdgcn_raw_buffer_load_b128(r, off, soff, AUX); }
__device__ __forceinline__ f32x4 ld_sc1(rsrc_t r, u32 off) { return __builtin_bit_cast(f32x4, __builtin_amdgcn_raw_buffer_load_b128(r, off, 0, 16)); }
__device__ __forceinline__ void st_sc1(rsrc_t r, u32 off, f32x4 v) { __builtin_amdgcn_raw_buffer_store_b128(__builtin_bit_cast(u32x4, v), r, off, 0, 16); }
__device__ __forceinline__ void wait_cnt(const u32* cnt, u32 want) {
#pragma nounroll
    for (u32 spin = 0; spin < SPIN_MAX; ++spin) {
        const u32 v = __hip_atomic_load(cnt, __ATOMIC_RELAXED, __HIP_MEMORY_SCOPE_AGENT);
        if ((u32)__builtin_amdgcn_readfirstlane(v) >= want) break;
        __builtin_amdgcn_s_sleep(POLL_SLEEP);
    }
    asm volatile("" ::: "memory");
}

__global__ void __launch_bounds__(256) k0_qproj(const uint16_t* __restrict__ xA, const uint16_t* __restrict__ wqA,
                                               const uint16_t* __restrict__ xB, const uint16_t* __restrict__ wqB, unsigned char* __restrict__ ws) {
    __shared__ __attribute__((aligned(16))) float red[4][128];
    const u64 t_start = __builtin_amdgcn_s_memrealtime();
    const int b = blockIdx.x, ab = b / (NH * QS), h = (b / QS) % NH, qs = b % QS;
    const int tid = threadIdx.x, lane = tid & 63, wave = tid >> 6, r = lane >> 4, c = lane & 15;
    const uint16_t* x = ab ? xB : xA;
    const rsrc_t rw = mkrsrc(ab ? wqB : wqA, DM * HD * 2);
    constexpr int RPW = DM / QS, NP = RPW / 16;
    const int row0 = qs * RPW + wave * 4 + r;
    u32x4 w[NP]; float xs[NP];
#pragma unroll
    for (int i = 0; i < NP; ++i) { w[i] = bld<2>(rw, ((row0 + i * 16) * HD + h * DH + 8 * c) * 2); xs[i] = __uint_as_float((u32)x[row0 + i * 16] << 16); }
    if (b == 0 && tid < NCNT) *(u32*)(ws + CNT_OFF + tid * 128) = 0u;
    if (b == 0 && tid == 0) { u64* T = (u64*)(ws + (8u << 20)); T[0] = t_start; T[1] = ~0ull; T[2] = 0; }
    float q[8];
#pragma unroll
    for (int k = 0; k < 8; ++k) q[k] = 0.f;
#pragma unroll
    for (int i = 0; i < NP; ++i)
#pragma unroll
        for (int k = 0; k < 4; ++k) { q[2 * k] = fmaf(xs[i], lo16(w[i][k]), q[2 * k]); q[2 * k + 1] = fmaf(xs[i], hi16(w[i][k]), q[2 * k + 1]); }
#pragma unroll
    for (int k = 0; k < 8; ++k) q[k] = xrow_sum(q[k]);
    if (r == 0) { *(f32x4*)&red[wave][8 * c] = f32x4{q[0], q[1], q[2], q[3]}; *(f32x4*)&red[wave][8 * c + 4] = f32x4{q[4], q[5], q[6], q[7]}; }
    __syncthreads();
    if (tid < 128) ((float*)(ws + Q_OFF))[(size_t)b * 128 + tid] = (red[0][tid] + red[1][tid]) + (red[2][tid] + red[3][tid]);
}

__global__ void __launch_bounds__(256, 7) fused(const uint16_t* __restrict__ kA, const uint16_t* __restrict__ vA, const uint16_t* __restrict__ woA,
                                                const uint16_t* __restrict__ kB, const uint16_t* __restrict__ vB, const uint16_t* __restrict__ woB,
                                                unsigned char* __restrict__ ws, uint16_t* __restrict__ out) {
    __shared__ __attribute__((aligned(16))) float red[8][128];
    __shared__ __attribute__((aligned(16))) float vec[128];
    __shared__ float mls[4][2];
    const int b = blockIdx.x;
    const int tid = threadIdx.x, lane = tid & 63, wave = __builtin_amdgcn_readfirstlane(tid >> 6), r = lane >> 4, c = lane & 15;
    const rsrc_t rws = mkrsrc(ws, WS_BYTES);
    u64* T = (u64*)(ws + (8u << 20));
    if (threadIdx.x == 0) __hip_atomic_fetch_min(T + 1, (u64)__builtin_amdgcn_s_memrealtime(), __ATOMIC_RELAXED, __HIP_MEMORY_SCOPE_AGENT);

    if (b >= NHELP) {
        const int a = b - NHELP;
        const bool isA = a < NH * NSA;
        const int h = isA ? a / NSA : (a - NH * NSA) / NSB;
        const int key0 = isA ? (a % NSA) * KPW : (SK - WIN) + ((a - NH * NSA) % NSB) * KPW;
        const rsrc_t rk = mkrsrc(isA ? kA : kB, NH * SK * DH * 2), rv = mkrsrc(isA ? vA : vB, NH * SK * DH * 2);
        const u32 off = ((h * SK + key0 + wave * 64 + r) * DH + 8 * c) * 2;
        u32x4 q4;
        const float* qp = (const float*)(ws + Q_OFF) + (size_t)((isA ? 0 : NH) + h) * QS * 128 + 8 * c;
        f32x4 q0[QS], q1[QS];
#pragma unroll
        for (int s2 = 0; s2 < QS; ++s2) { q0[s2] = *(const f32x4*)(qp + s2 * 128); q1[s2] = *(const f32x4*)(qp + s2 * 128 + 4); }
        u32x4 buf[32];
#define ULD(n) (((n) & 8) ? bld<2>(rv, off, (((n) >> 4) * 8 + ((n) & 7)) * 1024) : bld<2>(rk, off, (((n) >> 4) * 8 + ((n) & 7)) * 1024))
#pragma unroll
        for (int n = 0; n < DEPTH; ++n) buf[n] = ULD(n);
        {
            const f32x4 qa = (q0[0] + q0[1]) + (q0[2] + q0[3]), qb = (q1[0] + q1[1]) + (q1[2] + q1[3]);
            q4 = u32x4{pack2(qa[0], qa[1]), pack2(qa[2], qa[3]), pack2(qb[0], qb[1]), pack2(qb[2], qb[3])};
        }
        float sc[8], m = -INFINITY, mc = -INFINITY, l = 0.f, acc[8];
#pragma unroll
        for (int k = 0; k < 8; ++k) acc[k] = 0.f;
#pragma unroll
        for (int n = 0; n < 32; ++n) {
            if ((n & 8) == 0) {
                float d = dot2(buf[n][0], q4[0], 0.f); d = dot2(buf[n][1], q4[1], d); d = dot2(buf[n][2], q4[2], d); d = dot2(buf[n][3], q4[3], d);
                d = row16_sum(d);
                d = bf16r(bf16r(d) * SCALE) * LOG2E;
                sc[n & 7] = d; mc = fmaxf(mc, d);
                if ((n & 7) == 7) {
                    mc = xrow_max(mc);
                    if (n > 8) {
                        const float mn = fmaxf(m, mc), f = __builtin_amdgcn_exp2f(m - mn);
                        l *= f;
#pragma unroll
                        for (int k = 0; k < 8; ++k) acc[k] *= f;
                        m = mn;
                    } else m = mc;
                }
            } else {
                const float p = __builtin_amdgcn_exp2f(sc[n & 7] - m);
                l += p;
#pragma unroll
                for (int k = 0; k < 4; ++k) { acc[2 * k] = fmaf(p, lo16(buf[n][k]), acc[2 * k]); acc[2 * k + 1] = fmaf(p, hi16(buf[n][k]), acc[2 * k + 1]); }
            }
            __builtin_amdgcn_sched_barrier(0);
            if (n + DEPTH < 32) buf[n + DEPTH] = ULD(n + DEPTH);
            __builtin_amdgcn_sched_barrier(0);
        }
#pragma unroll
        for (int k = 0; k < 8; ++k) acc[k] = xrow_sum(acc[k]);
        l = xrow_sum(l);
        if (r == 0) {
            *(f32x4*)&red[wave][8 * c] = f32x4{acc[0], acc[1], acc[2], acc[3]};
            *(f32x4*)&red[wave][8 * c + 4] = f32x4{acc[4], acc[5], acc[6], acc[7]};
            if (c == 0) { mls[wave][0] = m; mls[wave][1] = l; }
        }
        __syncthreads();
        if (threadIdx.x == 0) __hip_atomic_fetch_max(T + 2, (u64)__builtin_amdgcn_s_memrealtime(), __ATOMIC_RELAXED, __HIP_MEMORY_SCOPE_AGENT);
        if (wave == 0) {
            const float M = fmaxf(fmaxf(mls[0][0], mls[1][0]), fmaxf(mls[2][0], mls[3][0]));
            f32x4 o = {0.f, 0.f, 0.f, 0.f}; float L = 0.f;
#pragma unroll
            for (int w = 0; w < 4; ++w) {
                const float f = __builtin_amdgcn_exp2f(mls[w][0] - M);
                o += f * *(const f32x4*)&red[w][4 * (lane & 31)];
                L = fmaf(f, mls[w][1], L);
            }
            if (lane == 32) o = f32x4{M, L, 0.f, 0.f};
            if (lane <= 32) st_sc1(rws, P_OFF + a * PSTR + lane * 16, o);
            asm volatile("s_waitcnt vmcnt(0)" ::: "memory");
            if (lane == 0) __hip_atomic_fetch_add((u32*)(ws + CNT_OFF + ((isA ? 0 : NH) + h) * 128), 1u, __ATOMIC_RELAXED, __HIP_MEMORY_SCOPE_AGENT);
        }
        return;
    }
    const int ab = b / (NH * NC5), h = (b / NC5) % NH, c5 = b % NC5;
    const int rg = tid >> 4;
    u32x4 wo[8];
    {
        const rsrc_t ro = mkrsrc(ab ? woB : woA, HD * DM * 2);
#pragma unroll
        for (int i = 0; i < 8; ++i) wo[i] = bld<2>(ro, ((h * DH + rg + 16 * i) * DM + c5 * 128 + 8 * c) * 2);
    }
    const int ns = ab ? NSB : NSA;
    const u32 pbase = P_OFF + (ab ? NH * NSA + h * NSB : h * NSA) * PSTR;
    if (wave == 0) wait_cnt((const u32*)(ws + CNT_OFF + (ab * NH + h) * 128), ns);
    __syncthreads();
    {
        const int half = lane >> 5, l32 = lane & 31, s0 = wave * 2 + half;
        f32x4 g[4];
        const f32x4 hd = ld_sc1(rws, pbase + (lane < ns ? lane : 0) * PSTR + 512);
        if (ab == 0) {
#pragma unroll
            for (int i = 0; i < 4; ++i) g[i] = ld_sc1(rws, pbase + (s0 + 8 * i) * PSTR + l32 * 16);
        } else g[0] = ld_sc1(rws, pbase + s0 * PSTR + l32 * 16);
        const float mm = lane < ns ? hd[0] : -INFINITY, ll = lane < ns ? hd[1] : 0.f;
        float M = mm;
#pragma unroll
        for (int o = 32; o >= 1; o >>= 1) M = fmaxf(M, __shfl_xor(M, o));
        const float f = __builtin_amdgcn_exp2f(mm - M);
        float L = f * ll;
#pragma unroll
        for (int o = 32; o >= 1; o >>= 1) L += __shfl_xor(L, o);
        const float wt = f / L;
        f32x4 o = __shfl(wt, s0) * g[0];
        if (ab == 0) {
#pragma unroll
            for (int i = 1; i < 4; ++i) o += __shfl(wt, s0 + 8 * i) * g[i];
        }
        *(f32x4*)&red[s0][4 * l32] = o;
    }
    __syncthreads();
    if (tid < 128) vec[tid] = bf16r(((red[0][tid] + red[1][tid]) + (red[2][tid] + red[3][tid])) + ((red[4][tid] + red[5][tid]) + (red[6][tid] + red[7][tid])));
    __syncthreads();
    {
        float acc[8];
#pragma unroll
        for (int k = 0; k < 8; ++k) acc[k] = 0.f;
#pragma unroll
        for (int i = 0; i < 8; ++i) {
            const float ov = vec[rg + 16 * i];
#pragma unroll
            for (int k = 0; k < 4; ++k) { acc[2 * k] = fmaf(ov, lo16(wo[i][k]), acc[2 * k]); acc[2 * k + 1] = fmaf(ov, hi16(wo[i][k]), acc[2 * k + 1]); }
        }
#pragma unroll
        for (int k = 0; k < 8; ++k) acc[k] = xrow_sum(acc[k]);
        if (r == 0) { *(f32x4*)&red[wave][8 * c] = f32x4{acc[0], acc[1], acc[2], acc[3]}; *(f32x4*)&red[wave][8 * c + 4] = f32x4{acc[4], acc[5], acc[6], acc[7]}; }
    }
    __syncthreads();
    const u32 ybase = Y_OFF + (ab * NC5 + c5) * NH * 512;
    u32* fcnt = (u32*)(ws + CNT_OFF + (2 * NH + ab * NC5 + c5) * 128);
    if (wave == 0) {
        if (lane < 32) {
            const f32x4 y = (*(const f32x4*)&red[0][4 * lane] + *(const f32x4*)&red[1][4 * lane]) + (*(const f32x4*)&red[2][4 * lane] + *(const f32x4*)&red[3][4 * lane]);
            st_sc1(rws, ybase + h * 512 + lane * 16, y);
        }
        asm volatile("s_waitcnt vmcnt(0)" ::: "memory");
        if (lane == 0) __hip_atomic_fetch_add(fcnt, 1u, __ATOMIC_RELAXED, __HIP_MEMORY_SCOPE_AGENT);
    }
    if (h != ab * NC5 + c5) return;
    if (wave == 0) wait_cnt(fcnt, NH);
    __syncthreads();
    {
        const int half = lane >> 5, l32 = lane & 31, s0 = wave * 2 + half;
        f32x4 g[4];
#pragma unroll
        for (int i = 0; i < 4; ++i) g[i] = ld_sc1(rws, ybase + (s0 + 8 * i) * 512 + l32 * 16);
        *(f32x4*)&red[s0][4 * l32] = (g[0] + g[1]) + (g[2] + g[3]);
    }
    __syncthreads();
    if (tid < 128) out[ab * DM + c5 * 128 + tid] = (uint16_t)(__float_as_uint(bf16r(((red[0][tid] + red[1][tid]) + (red[2][tid] + red[3][tid])) + ((red[4][tid] + red[5][tid]) + (red[6][tid] + red[7][tid])))) >> 16);
    if (tid == 0 && ab == 0 && c5 == 0) { const u64 te = __builtin_amdgcn_s_memrealtime(); const u64 a0 = __hip_atomic_load(T, __ATOMIC_RELAXED, __HIP_MEMORY_SCOPE_AGENT), a1 = __hip_atomic_load(T + 1, __ATOMIC_RELAXED, __HIP_MEMORY_SCOPE_AGENT), a2 = __hip_atomic_load(T + 2, __ATOMIC_RELAXED, __HIP_MEMORY_SCOPE_AGENT);
      printf("SPAN k0->fused %d att_end %d fin %d\n", (int)(a1 - a0), (int)(a2 - a0), (int)(te - a0)); }
}

extern "C" void kernel_launch(void* const* d_in, const int* in_sizes, int n_in,
                              void* d_out, int out_size, void* d_ws, size_t ws_size,
                              hipStream_t stream) {
    k0_qproj<<<2 * NH * QS, 256, 0, stream>>>((const uint16_t*)d_in[0], (const uint16_t*)d_in[3], (const uint16_t*)d_in[5], (const uint16_t*)d_in[8], (unsigned char*)d_ws);
    fused<<<NWG, 256, 0, stream>>>((const uint16_t*)d_in[1], (const uint16_t*)d_in[2], (const uint16_t*)d_in[4],
                                   (const uint16_t*)d_in[6], (const uint16_t*)d_in[7], (const uint16_t*)d_in[9],
                                   (unsigned char*)d_ws, (uint16_t*)d_out);
}
```

```cpp
#include <hip/hip_runtime.h>
#include <hip/hip_bf16.h>
#include <stdint.h>

typedef unsigned int u32;
typedef unsigned long long u64;
typedef __attribute__((ext_vector_type(4))) unsigned int u32x4;
typedef __attribute__((ext_vector_type(2))) unsigned int u32x2;
typedef __attribute__((ext_vector_type(4))) float f32x4;
typedef __attribute__((ext_vector_type(2))) float f32x2;
typedef __attribute__((ext_vector_type(2))) __bf16 bf16x2;
typedef __amdgpu_buffer_rsrc_t rsrc_t;

constexpr int NH = 32, SK = 8192, DH = 128, DM = 640, WIN = 2048, HD = NH * DH;
constexpr float SCALE = 0.08838834764831845f;
constexpr float LOG2E = 1.4426950408889634f;
constexpr int KPW = 256;
constexpr int NSA = SK / KPW, NSB = WIN / KPW;
constexpr int NATT = NH * (NSA + NSB);
constexpr int NC5 = 5;
constexpr int NHELP = 2 * NH * NC5;
constexpr int NWG = NHELP + NATT;
#define DEPTH 6
#define POLL_SLEEP 8
#define PRE_SLEEP 32
#define QPOLL_SLEEP 4
constexpr u32 SPIN_MAX = 1u << 20;
constexpr int NG = 43;
constexpr int NS = 48;
constexpr u32 VSTR = 768;
constexpr u32 E_OFF = 0;
constexpr u32 Q_OFF = 16384;
constexpr u32 H_OFF = Q_OFF + NHELP * VSTR;
constexpr u32 P_OFF = H_OFF + NATT * 16;
constexpr u32 Y_OFF = P_OFF + NATT * VSTR;
constexpr u32 WS_BYTES = Y_OFF + NHELP * VSTR;

__device__ __forceinline__ float dot2(u32 a, u32 b, float c) {
    return __builtin_amdgcn_fdot2_f32_bf16(__builtin_bit_cast(bf16x2, a), __builtin_bit_cast(bf16x2, b), c, false);
}
template <int CTRL> __device__ __forceinline__ float dpp(float x) {
    return __builtin_bit_cast(float, __builtin_amdgcn_mov_dpp(__builtin_bit_cast(int, x), CTRL, 0xf, 0xf, true));
}
constexpr int XOR1 = 0xB1, XOR2 = 0x4E, HMIR = 0x141, MIR = 0x140;
__device__ __forceinline__ float row16_sum(float x) {
    x += dpp<XOR1>(x); x += dpp<XOR2>(x); x += dpp<HMIR>(x); x += dpp<MIR>(x); return x;
}
__device__ __forceinline__ float xrow_sum(float x) {
    auto s = __builtin_amdgcn_permlane16_swap(__float_as_uint(x), __float_as_uint(x), false, false);
    x = __uint_as_float(s[0]) + __uint_as_float(s[1]);
    auto t = __builtin_amdgcn_permlane32_swap(__float_as_uint(x), __float_as_uint(x), false, false);
    return __uint_as_float(t[0]) + __uint_as_float(t[1]);
}
__device__ __forceinline__ float xrow_max(float x) {
    auto s = __builtin_amdgcn_permlane16_swap(__float_as_uint(x), __float_as_uint(x), false, false);
    x = fmaxf(__uint_as_float(s[0]), __uint_as_float(s[1]));
    auto t = __builtin_amdgcn_permlane32_swap(__float_as_uint(x), __float_as_uint(x), false, false);
    return fmaxf(__uint_as_float(t[0]), __uint_as_float(t[1]));
}
__device__ __forceinline__ float bf16r(float x) { return (float)(__bf16)x; }
__device__ __forceinline__ float lo16(u32 w) { return __uint_as_float(w << 16); }
__device__ __forceinline__ float hi16(u32 w) { return __uint_as_float(w & 0xffff0000u); }
__device__ __forceinline__ u32 pack2(float a, float b) { return (__float_as_uint(bf16r(a)) >> 16) | (__float_as_uint(bf16r(b)) & 0xffff0000u); }
__device__ __forceinline__ rsrc_t mkrsrc(const void* p, u32 bytes) { return __builtin_amdgcn_make_buffer_rsrc((void*)p, 0, bytes, 0x00020000); }
template <int AUX> __device__ __forceinline__ u32x4 bld(rsrc_t r, u32 off, u32 soff = 0) { return __builtin_amdgcn_raw_buffer_load_b128(r, off, soff, AUX); }
__device__ __forceinline__ u32x4 ld_sc1(rsrc_t r, u32 off, u32 soff = 0) { return __builtin_amdgcn_raw_buffer_load_b128(r, off, soff, 16); }
__device__ __forceinline__ void lds_barrier() { asm volatile("s_waitcnt lgkmcnt(0)\n\ts_barrier" ::: "memory"); }
__device__ __forceinline__ void st_sc1(rsrc_t r, u32 off, u32x4 v) { __builtin_amdgcn_raw_buffer_store_b128(v, r, off, 0, 16); }

struct Tag { u32 t0, t1, t2, t3; };
__device__ __forceinline__ Tag mktag(u64 e) {
    const u64 n = e + 1; const u32 hi = (u32)(n >> 32);
    return Tag{hi & 63u, (hi >> 6) & 63u, (hi >> 12) & 63u, (u32)n};
}
__device__ __forceinline__ u64 ld_epoch(const unsigned char* p) {
    const u64 v = __hip_atomic_load((const u64*)p, __ATOMIC_RELAXED, __HIP_MEMORY_SCOPE_AGENT);
    return ((u64)(u32)__builtin_amdgcn_readfirstlane((u32)(v >> 32)) << 32) | (u32)__builtin_amdgcn_readfirstlane((u32)v);
}
__device__ __forceinline__ float trunc6(float x) { return __uint_as_float(__float_as_uint(x) & ~63u); }
__device__ __forceinline__ u32x4 mkgran(float a, float b, float c, const Tag& t) {
    return u32x4{(__float_as_uint(a) & ~63u) | t.t0, (__float_as_uint(b) & ~63u) | t.t1, (__float_as_uint(c) & ~63u) | t.t2, t.t3};
}
__device__ __forceinline__ u32 gbad(u32x4 g, const Tag& t) { return (((g[0] ^ t.t0) | (g[1] ^ t.t1) | (g[2] ^ t.t2)) & 63u) | (g[3] ^ t.t3); }
__device__ __forceinline__ float gval(u32 w) { return __uint_as_float(w & ~63u); }

__global__ void __launch_bounds__(256, 7) fused(const uint16_t* __restrict__ xA, const uint16_t* __restrict__ kA, const uint16_t* __restrict__ vA,
                                                const uint16_t* __restrict__ wqA, const uint16_t* __restrict__ woA,
                                                const uint16_t* __restrict__ xB, const uint16_t* __restrict__ kB, const uint16_t* __restrict__ vB,
                                                const uint16_t* __restrict__ wqB, const uint16_t* __restrict__ woB,
                                                unsigned char* __restrict__ ws, uint16_t* __restrict__ out) {
    __shared__ __attribute__((aligned(16))) float raw[32][3 * NS];
    float* rawf = &raw[0][0];
    __shared__ __attribute__((aligned(16))) float red[4][128];
    __shared__ __attribute__((aligned(16))) float vec[128];
    __shared__ float mls[4][2];
    const int b = blockIdx.x;
    const int tid = threadIdx.x, lane = tid & 63, wave = __builtin_amdgcn_readfirstlane(tid >> 6), r = lane >> 4, c = lane & 15;
    const rsrc_t rws = mkrsrc(ws, WS_BYTES);

    if (b >= NHELP) {
        const int a = b - NHELP;
        const bool isA = a < NH * NSA;
        const int h = isA ? a / NSA : (a - NH * NSA) / NSB;
        const int hh = (isA ? 0 : NH) + h;
        const int key0 = isA ? (a % NSA) * KPW : (SK - WIN) + ((a - NH * NSA) % NSB) * KPW;
        const rsrc_t rk = mkrsrc(isA ? kA : kB, NH * SK * DH * 2), rv = mkrsrc(isA ? vA : vB, NH * SK * DH * 2);
        const u32 off = ((h * SK + key0 + wave * 64 + r) * DH + 8 * c) * 2;
        const u64 ev = __hip_atomic_load((const u64*)(ws + E_OFF + hh * 128), __ATOMIC_RELAXED, __HIP_MEMORY_SCOPE_AGENT);
        if (PRE_SLEEP) __builtin_amdgcn_s_sleep(PRE_SLEEP);
        u32x4 buf[32];
#define ULD(n) (((n) & 8) ? bld<2>(rv, off, (((n) >> 4) * 8 + ((n) & 7)) * 1024) : bld<2>(rk, off, (((n) >> 4) * 8 + ((n) & 7)) * 1024))
#pragma unroll
        for (int n = 0; n < DEPTH; ++n) buf[n] = ULD(n);
        const Tag tg = mktag(((u64)(u32)__builtin_amdgcn_readfirstlane((u32)(ev >> 32)) << 32) | (u32)__builtin_amdgcn_readfirstlane((u32)ev));
        {
            const int t2 = tid < NC5 * NS ? tid : tid - 64;
            u32 qoff = Q_OFF + hh * NC5 * VSTR + t2 * 16;
            u32x4 gq;
#pragma nounroll
            for (u32 spin = 0; spin < SPIN_MAX; ++spin) {
                asm volatile("" : "+v"(qoff));
                gq = ld_sc1(rws, qoff);
                if (__all(gbad(gq, tg) == 0)) break;
                __builtin_amdgcn_s_sleep(QPOLL_SLEEP);
            }
            rawf[3 * t2] = gval(gq[0]); rawf[3 * t2 + 1] = gval(gq[1]); rawf[3 * t2 + 2] = gval(gq[2]);
        }
        __syncthreads();
        u32x4 q4;
        {
            f32x4 qa = *(const f32x4*)&raw[0][8 * c], qb = *(const f32x4*)&raw[0][8 * c + 4];
#pragma unroll
            for (int p = 1; p < NC5; ++p) { qa += *(const f32x4*)&raw[p][8 * c]; qb += *(const f32x4*)&raw[p][8 * c + 4]; }
            q4 = u32x4{pack2(qa[0], qa[1]), pack2(qa[2], qa[3]), pack2(qb[0], qb[1]), pack2(qb[2], qb[3])};
        }
        float sc[8], m = -INFINITY, mc = -INFINITY, l = 0.f, acc[8];
#pragma unroll
        for (int k = 0; k < 8; ++k) acc[k] = 0.f;
#pragma unroll
        for (int n = 0; n < 32; ++n) {
            if ((n & 8) == 0) {
                float d = dot2(buf[n][0], q4[0], 0.f); d = dot2(buf[n][1], q4[1], d); d = dot2(buf[n][2], q4[2], d); d = dot2(buf[n][3], q4[3], d);
                d = row16_sum(d);
                d = bf16r(bf16r(d) * SCALE) * LOG2E;
                sc[n & 7] = d; mc = fmaxf(mc, d);
                if ((n & 7) == 7) {
                    mc = xrow_max(mc);
                    if (n > 8) {
                        const float mn = fmaxf(m, mc), f = __builtin_amdgcn_exp2f(m - mn);
                        l *= f;
#pragma unroll
                        for (int k = 0; k < 8; ++k) acc[k] *= f;
                        m = mn;
                    } else m = mc;
                }
            } else {
                const float p = __builtin_amdgcn_exp2f(sc[n & 7] - m);
                l += p;
#pragma unroll
                for (int k = 0; k < 4; ++k) { acc[2 * k] = fmaf(p, lo16(buf[n][k]), acc[2 * k]); acc[2 * k + 1] = fmaf(p, hi16(buf[n][k]), acc[2 * k + 1]); }
            }
            __builtin_amdgcn_sched_barrier(0);
            if (n + DEPTH < 32) buf[n + DEPTH] = ULD(n + DEPTH);
            __builtin_amdgcn_sched_barrier(0);
        }
#pragma unroll
        for (int k = 0; k < 8; ++k) acc[k] = xrow_sum(acc[k]);
        l = xrow_sum(l);
        if (r == 0) {
            *(f32x4*)&red[wave][8 * c] = f32x4{acc[0], acc[1], acc[2], acc[3]};
            *(f32x4*)&red[wave][8 * c + 4] = f32x4{acc[4], acc[5], acc[6], acc[7]};
            if (c == 0) { mls[wave][0] = m; mls[wave][1] = l; }
        }
        __syncthreads();
        if (wave == 0) {
            const float M = trunc6(fmaxf(fmaxf(mls[0][0], mls[1][0]), fmaxf(mls[2][0], mls[3][0])));
            const int g = lane < NG ? lane : 0, i2 = lane < NG - 1 ? 3 * g + 2 : 0;
            float o0 = 0.f, o1 = 0.f, o2 = 0.f, L = 0.f;
#pragma unroll
            for (int w = 0; w < 4; ++w) {
                const float f = __builtin_amdgcn_exp2f(mls[w][0] - M);
                o0 = fmaf(f, red[w][3 * g], o0); o1 = fmaf(f, red[w][3 * g + 1], o1); o2 = fmaf(f, red[w][i2], o2);
                L = fmaf(f, mls[w][1], L);
            }
            if (lane >= NG) { o0 = M; o1 = L; o2 = 0.f; }
            if (lane <= NS) st_sc1(rws, lane < NS ? P_OFF + a * VSTR + lane * 16 : H_OFF + a * 16, mkgran(o0, o1, o2, tg));
        }
        return;
    }
    const int ab = b / (NH * NC5), h = (b / NC5) % NH, c5 = b % NC5;
    const int hh = ab * NH + h;
    const int rg = tid >> 4;
    unsigned char* ep = ws + E_OFF + hh * 128;
    unsigned char* fp = ws + E_OFF + (2 * NH + ab * NC5 + c5) * 128;
    u64 ev, fv;
    u32x4 wo[8];
    {
        const uint16_t* x = ab ? xB : xA;
        const rsrc_t rw = mkrsrc(ab ? wqB : wqA, DM * HD * 2);
        const int row0 = c5 * 128 + rg;
        u32x4 w[8]; float xs[8];
#pragma unroll
        for (int i = 0; i < 8; ++i) w[i] = bld<2>(rw, (row0 * HD + h * DH + 8 * c) * 2, i * 16 * HD * 2);
#pragma unroll
        for (int i = 0; i < 8; ++i) xs[i] = __uint_as_float((u32)x[row0 + i * 16] << 16);
        ev = ld_epoch(ep); fv = ld_epoch(fp);
        float q[8];
#pragma unroll
        for (int k = 0; k < 8; ++k) q[k] = 0.f;
#pragma unroll
        for (int i = 0; i < 8; ++i)
#pragma unroll
            for (int k = 0; k < 4; ++k) { q[2 * k] = fmaf(xs[i], lo16(w[i][k]), q[2 * k]); q[2 * k + 1] = fmaf(xs[i], hi16(w[i][k]), q[2 * k + 1]); }
#pragma unroll
        for (int k = 0; k < 8; ++k) q[k] = xrow_sum(q[k]);
        if (r == 0) { *(f32x4*)&red[wave][8 * c] = f32x4{q[0], q[1], q[2], q[3]}; *(f32x4*)&red[wave][8 * c + 4] = f32x4{q[4], q[5], q[6], q[7]}; }
    }
    __builtin_amdgcn_sched_barrier(0);
    {
        const rsrc_t ro = mkrsrc(ab ? woB : woA, HD * DM * 2);
#pragma unroll
        for (int i = 0; i < 8; ++i) wo[i] = bld<2>(ro, ((h * DH + rg) * DM + c5 * 128 + 8 * c) * 2, i * 16 * DM * 2);
    }
    __builtin_amdgcn_sched_barrier(0);
    const Tag tg = mktag(ev), tf = mktag(fv);
    lds_barrier();
    if (wave == 0 && lane < NS) {
        const int i0 = lane < NG ? 3 * lane : 0, i2 = lane < NG - 1 ? 3 * lane + 2 : 0;
        const float q0 = (red[0][i0] + red[1][i0]) + (red[2][i0] + red[3][i0]);
        const float q1 = (red[0][i0 + 1] + red[1][i0 + 1]) + (red[2][i0 + 1] + red[3][i0 + 1]);
        const float q2 = (red[0][i2] + red[1][i2]) + (red[2][i2] + red[3][i2]);
        st_sc1(rws, Q_OFF + b * VSTR + lane * 16, mkgran(q0, q1, q2, tg));
    }
    const int npw = ab ? NSB / 4 : NSA / 4;
    const int a0 = (ab ? NH * NSA + h * NSB : h * NSA) + wave * npw;
    {
        u32 hoff = H_OFF + (a0 + (lane < npw ? lane : 0)) * 16;
        u32x4 hd;
#pragma nounroll
        for (u32 spin = 0; spin < SPIN_MAX; ++spin) {
            asm volatile("" : "+v"(hoff));
            hd = ld_sc1(rws, hoff);
            if (__all(gbad(hd, tg) == 0)) break;
            __builtin_amdgcn_s_sleep(POLL_SLEEP);
        }
        u32x4 g[6];
        u32 goff = P_OFF + a0 * VSTR + lane * 16;
        const u32 o1 = (ab && lane >= 32) ? 0u : 1024u;
#pragma nounroll
        for (u32 spin = 0; spin < SPIN_MAX; ++spin) {
            u32 bad = 0;
            asm volatile("" : "+v"(goff));
#pragma unroll
            for (int i = 0; i < 6; ++i) { if (i == 1) g[i] = ld_sc1(rws, goff + o1); else if (i < 2 || ab == 0) g[i] = ld_sc1(rws, goff, i * 1024); }
#pragma unroll
            for (int i = 0; i < 6; ++i) if (i < 2 || ab == 0) bad |= gbad(g[i], tg);
            if (__all(bad == 0)) break;
            __builtin_amdgcn_s_sleep(1);
        }
#pragma unroll
        for (int i = 0; i < 6; ++i) {
            if (i < 2 || ab == 0) { float* d = &rawf[3 * (wave * 8 * NS + lane + (i == 1 ? (int)(o1 >> 4) : i * 64))]; d[0] = gval(g[i][0]); d[1] = gval(g[i][1]); d[2] = gval(g[i][2]); }
        }
        const float mm = lane < npw ? gval(hd[0]) : -INFINITY, ll = lane < npw ? gval(hd[1]) : 0.f;
        float M = mm;
        M = fmaxf(M, dpp<XOR1>(M)); M = fmaxf(M, dpp<XOR2>(M)); M = fmaxf(M, dpp<HMIR>(M));
        M = __builtin_bit_cast(float, __builtin_amdgcn_readfirstlane(__builtin_bit_cast(int, M)));
        const float f = __builtin_amdgcn_exp2f(mm - M);
        float L = f * ll;
        L += dpp<XOR1>(L); L += dpp<XOR2>(L); L += dpp<HMIR>(L);
        f32x2 o = {0.f, 0.f};
        __builtin_amdgcn_s_waitcnt(0xc07f);
        __builtin_amdgcn_wave_barrier();
#pragma unroll
        for (int j = 0; j < 8; ++j) {
            if (j < 2 || ab == 0) {
                const float fj = __builtin_bit_cast(float, __builtin_amdgcn_readlane(__builtin_bit_cast(int, f), j));
                o += fj * *(const f32x2*)&raw[wave * 8 + j][2 * lane];
            }
        }
        *(f32x2*)&raw[wave * 8][2 * lane] = o;
        if (lane == 0) { mls[wave][0] = M; mls[wave][1] = L; }
    }
    __syncthreads();
    if (c5 == 0 && tid == 0) __hip_atomic_store((u64*)ep, ev + 1, __ATOMIC_RELAXED, __HIP_MEMORY_SCOPE_AGENT);
    if (tid < 128) {
        const float M = fmaxf(fmaxf(mls[0][0], mls[1][0]), fmaxf(mls[2][0], mls[3][0]));
        float o = 0.f, L = 0.f;
#pragma unroll
        for (int w = 0; w < 4; ++w) { const float f = __builtin_amdgcn_exp2f(mls[w][0] - M); o = fmaf(f, raw[w * 8][tid], o); L = fmaf(f, mls[w][1], L); }
        vec[tid] = bf16r(o / L);
    }
    __syncthreads();
    {
        float acc[8];
#pragma unroll
        for (int k = 0; k < 8; ++k) acc[k] = 0.f;
#pragma unroll
        for (int i = 0; i < 8; ++i) {
            const float ov = vec[rg + 16 * i];
#pragma unroll
            for (int k = 0; k < 4; ++k) { acc[2 * k] = fmaf(ov, lo16(wo[i][k]), acc[2 * k]); acc[2 * k + 1] = fmaf(ov, hi16(wo[i][k]), acc[2 * k + 1]); }
        }
#pragma unroll
        for (int k = 0; k < 8; ++k) acc[k] = xrow_sum(acc[k]);
        if (r == 0) { *(f32x4*)&red[wave][8 * c] = f32x4{acc[0], acc[1], acc[2], acc[3]}; *(f32x4*)&red[wave][8 * c + 4] = f32x4{acc[4], acc[5], acc[6], acc[7]}; }
    }
    __syncthreads();
    const u32 ybase = Y_OFF + (ab * NC5 + c5) * NH * VSTR;
    if (wave == 0 && lane < NS) {
        const int i0 = lane < NG ? 3 * lane : 0, i2 = lane < NG - 1 ? 3 * lane + 2 : 0;
        const float y0 = (red[0][i0] + red[1][i0]) + (red[2][i0] + red[3][i0]);
        const float y1 = (red[0][i0 + 1] + red[1][i0 + 1]) + (red[2][i0 + 1] + red[3][i0 + 1]);
        const float y2 = (red[0][i2] + red[1][i2]) + (red[2][i2] + red[3][i2]);
        st_sc1(rws, ybase + h * VSTR + lane * 16, mkgran(y0, y1, y2, tf));
    }
    if (h != ab * NC5 + c5) return;
    {
        u32x4 g[6];
        u32 goff = ybase + wave * 8 * VSTR + lane * 16;
#pragma nounroll
        for (u32 spin = 0; spin < SPIN_MAX; ++spin) {
            u32 bad = 0;
            asm volatile("" : "+v"(goff));
#pragma unroll
            for (int i = 0; i < 6; ++i) g[i] = ld_sc1(rws, goff, i * 1024);
#pragma unroll
            for (int i = 0; i < 6; ++i) bad |= gbad(g[i], tf);
            if (__all(bad == 0)) break;
            __builtin_amdgcn_s_sleep(1);
        }
#pragma unroll
        for (int i = 0; i < 6; ++i) {
            float* d = &rawf[3 * (wave * 8 * NS + lane + i * 64)]; d[0] = gval(g[i][0]); d[1] = gval(g[i][1]); d[2] = gval(g[i][2]);
        }
    }
    __syncthreads();
    if (tid == 128) __hip_atomic_store((u64*)fp, fv + 1, __ATOMIC_RELAXED, __HIP_MEMORY_SCOPE_AGENT);
    if (tid < 128) {
        float s0 = 0.f, s1 = 0.f, s2 = 0.f, s3 = 0.f;
#pragma unroll
        for (int j = 0; j < 8; ++j) { s0 += raw[j][tid]; s1 += raw[8 + j][tid]; s2 += raw[16 + j][tid]; s3 += raw[24 + j][tid]; }
        out[ab * DM + c5 * 128 + tid] = (uint16_t)(__float_as_uint(bf16r((s0 + s1) + (s2 + s3))) >> 16);
    }
}

extern "C" void kernel_launch(void* const* d_in, const int* in_sizes, int n_in,
                              void* d_out, int out_size, void* d_ws, size_t ws_size,
                              hipStream_t stream) {
    fused<<<NWG, 256, 0, stream>>>((const uint16_t*)d_in[0], (const uint16_t*)d_in[1], (const uint16_t*)d_in[2], (const uint16_t*)d_in[3], (const uint16_t*)d_in[4],
                                   (const uint16_t*)d_in[5], (const uint16_t*)d_in[6], (const uint16_t*)d_in[7], (const uint16_t*)d_in[8], (const uint16_t*)d_in[9],
                                   (unsigned char*)d_ws, (uint16_t*)d_out);
}
```

```cpp
#include <hip/hip_runtime.h>
#include <hip/hip_bf16.h>
#include <stdint.h>

typedef unsigned int u32;
typedef unsigned long long u64;
typedef __attribute__((ext_vector_type(4))) unsigned int u32x4;
typedef __attribute__((ext_vector_type(2))) unsigned int u32x2;
typedef __attribute__((ext_vector_type(4))) float f32x4;
typedef __attribute__((ext_vector_type(2))) float f32x2;
typedef __attribute__((ext_vector_type(2))) __bf16 bf16x2;
typedef __amdgpu_buffer_rsrc_t rsrc_t;

constexpr int NH = 32, SK = 8192, DH = 128, DM = 640, WIN = 2048, HD = NH * DH;
constexpr float SCALE = 0.08838834764831845f;
constexpr float LOG2E = 1.4426950408889634f;
constexpr int NWG = 256, NW = 16;
constexpr int NL = 40;
constexpr int NRED = 160;
#define PF 16
#define PFV 16
#define FA1 0
#define FA2 12
#define FA3 24
#define PW 2
#define NPROBE 4
#define WMIX 0
#define XDBG 0
#define SAUX 16
#define PRET 0
#define PAUX 16
#define QUIET 0
#define PCAP 200
#define GSLEEP 4
#define QSL 4
#define SSL 4
constexpr u32 SPOLL = 64;
constexpr u32 SPIN_MAX = 1u << 20;
constexpr u32 E_OFF = 0;
constexpr u32 G_OFF = 2048;
constexpr u32 Q_OFF = 16384;
constexpr u32 R_OFF = Q_OFF + 32 * 1024;
constexpr u32 Y_OFF = R_OFF + 2 * 32 * 2 * 3072;
constexpr u32 D_OFF = Y_OFF + NRED * 64 * 64;
constexpr u32 S_OFF = D_OFF + 32 * 128;
constexpr u32 WS_BYTES = S_OFF + 32 * 8 * 16 * 256;

__device__ __forceinline__ float dot2(u32 a, u32 b, float c) {
    return __builtin_amdgcn_fdot2_f32_bf16(__builtin_bit_cast(bf16x2, a), __builtin_bit_cast(bf16x2, b), c, false);
}
template <int CTRL> __device__ __forceinline__ float dpp(float x) {
    return __builtin_bit_cast(float, __builtin_amdgcn_mov_dpp(__builtin_bit_cast(int, x), CTRL, 0xf, 0xf, true));
}
constexpr int XOR1 = 0xB1, XOR2 = 0x4E, HMIR = 0x141, MIR = 0x140, ROR4 = 0x124, ROR8 = 0x128;
__device__ __forceinline__ float row16_sum(float x) {
    x += dpp<XOR1>(x); x += dpp<XOR2>(x); x += dpp<HMIR>(x); x += dpp<MIR>(x); return x;
}
__device__ __forceinline__ float row16_max(float x) {
    x = fmaxf(x, dpp<XOR1>(x)); x = fmaxf(x, dpp<XOR2>(x)); x = fmaxf(x, dpp<HMIR>(x)); x = fmaxf(x, dpp<MIR>(x)); return x;
}
__device__ __forceinline__ float sw16_sum(float x) {
    auto s = __builtin_amdgcn_permlane16_swap(__float_as_uint(x), __float_as_uint(x), false, false);
    return __uint_as_float(s[0]) + __uint_as_float(s[1]);
}
__device__ __forceinline__ float sw32_sum2(float a, float b) {
    auto t = __builtin_amdgcn_permlane32_swap(__float_as_uint(a), __float_as_uint(b), false, false);
    return __uint_as_float(t[0]) + __uint_as_float(t[1]);
}
__device__ __forceinline__ float xrow_sum(float x) { x = sw16_sum(x); return sw32_sum2(x, x); }
__device__ __forceinline__ float xrow_max(float x) {
    auto s = __builtin_amdgcn_permlane16_swap(__float_as_uint(x), __float_as_uint(x), false, false);
    x = fmaxf(__uint_as_float(s[0]), __uint_as_float(s[1]));
    auto t = __builtin_amdgcn_permlane32_swap(__float_as_uint(x), __float_as_uint(x), false, false);
    return fmaxf(__uint_as_float(t[0]), __uint_as_float(t[1]));
}
__device__ __forceinline__ float rdlane(float x, int l) { return __builtin_bit_cast(float, __builtin_amdgcn_readlane(__builtin_bit_cast(int, x), l)); }
__device__ __forceinline__ float bf16r(float x) { return (float)(__bf16)x; }
__device__ __forceinline__ float lo16(u32 w) { return __uint_as_float(w << 16); }
__device__ __forceinline__ float hi16(u32 w) { return __uint_as_float(w & 0xffff0000u); }
__device__ __forceinline__ rsrc_t mkrsrc(const void* p, u32 bytes) { return __builtin_amdgcn_make_buffer_rsrc((void*)p, 0, bytes, 0x00020000); }
template <int AUX> __device__ __forceinline__ u32x4 bld(rsrc_t r, u32 off, u32 soff = 0) { return __builtin_amdgcn_raw_buffer_load_b128(r, off, soff, AUX); }
__device__ __forceinline__ u32x4 ld_sc1(rsrc_t r, u32 off, u32 soff = 0) { return __builtin_amdgcn_raw_buffer_load_b128(r, off, soff, 16); }
__device__ __forceinline__ void st_sc1(rsrc_t r, u32 off, u32x4 v) { __builtin_amdgcn_raw_buffer_store_b128(v, r, off, 0, 16); }
__device__ __forceinline__ void lds_barrier() { asm volatile("s_waitcnt lgkmcnt(0)\n\ts_barrier" ::: "memory"); }
__device__ __forceinline__ void lds_wait() { asm volatile("s_waitcnt lgkmcnt(0)" ::: "memory"); }
template <int N> __device__ __forceinline__ void vm_wait() { __builtin_amdgcn_s_waitcnt(0x0f70 | (N & 15) | ((N >> 4) << 14)); }
__device__ __forceinline__ u64 uni64(u64 v) { return ((u64)(u32)__builtin_amdgcn_readfirstlane((u32)(v >> 32)) << 32) | (u32)__builtin_amdgcn_readfirstlane((u32)v); }

struct Tag { u32 t0, t1, t2, t3; };
__device__ __forceinline__ Tag mktag(u64 e) {
    const u64 n = e + 1; const u32 hi = (u32)(n >> 32);
    return Tag{hi & 63u, (hi >> 6) & 63u, (hi >> 12) & 63u, (u32)n};
}
__device__ __forceinline__ float trunc6(float x) { return __uint_as_float(__float_as_uint(x) & ~63u); }
__device__ __forceinline__ u32x4 mkgran(float a, float b, float c, const Tag& t) {
    return u32x4{(__float_as_uint(a) & ~63u) | t.t0, (__float_as_uint(b) & ~63u) | t.t1, (__float_as_uint(c) & ~63u) | t.t2, t.t3};
}
__device__ __forceinline__ u32 gbad(u32x4 g, const Tag& t) { return (((g[0] ^ t.t0) | (g[1] ^ t.t1) | (g[2] ^ t.t2)) & 63u) | (g[3] ^ t.t3); }
__device__ __forceinline__ float gval(u32 w) { return __uint_as_float(w & ~63u); }
__device__ __forceinline__ u32x4 gran8(float v0, float v1, float v2, float v3, float v4, float v5, float v6, float v7, int rr, const Tag& t) {
    const float a = rr == 0 ? v0 : rr == 1 ? v3 : v6, b = rr == 0 ? v1 : rr == 1 ? v4 : v7, c = rr == 0 ? v2 : v5;
    return mkgran(a, b, c, t);
}
#define GRAN8(v, rr, t) gran8(v[0], v[1], v[2], v[3], v[4], v[5], v[6], v[7], rr, t)

typedef __attribute__((ext_vector_type(16))) unsigned int u32x16;
__device__ __forceinline__ u64 sld64(const void* p) { u64 v; asm volatile("s_load_dwordx2 %0, %1, 0x0 glc\n\ts_waitcnt lgkmcnt(0)" : "=&s"(v) : "s"(p) : "memory"); return v; }
__device__ __forceinline__ u32x16 sld16(const void* p) { u32x16 v; asm volatile("s_load_dwordx16 %0, %1, 0x0 glc\n\ts_waitcnt lgkmcnt(0)" : "=&s"(v) : "s"(p) : "memory"); return v; }
__device__ __forceinline__ u32 tbad(const u32x16& d, u32 hi, u32 lo) {
    return ((d[2] ^ hi) | (d[3] ^ lo)) | ((d[6] ^ hi) | (d[7] ^ lo)) | ((d[10] ^ hi) | (d[11] ^ lo)) | ((d[14] ^ hi) | (d[15] ^ lo));
}
__device__ __forceinline__ u32 wrlane(u32 v, int l, u32 old) { asm("v_writelane_b32 %0, %1, %2" : "+v"(old) : "s"(v), "n"(l)); return old; }
__device__ __forceinline__ void glds16(const void* gsrc, u32 lds_dst) {
    u32 keep;
    asm volatile("s_mov_b32 %0, m0\n\ts_mov_b32 m0, %2\n\ts_nop 0\n\tglobal_load_lds_dwordx4 %1, off nt\n\ts_mov_b32 m0, %0" : "=&s"(keep) : "v"(gsrc), "s"(lds_dst) : "memory");
}
__device__ __forceinline__ u32 lds_ld(const u32* p) { return __builtin_amdgcn_readfirstlane(__hip_atomic_load(p, __ATOMIC_RELAXED, __HIP_MEMORY_SCOPE_WORKGROUP)); }

struct Smem {
    u32x4 wo[40 * 64];
    float part[2][NW][128];
    float ml[2][NW][2];
    float comb[2][132];
    float tsl[8 * 72];
    float ov[64];
    float qp[4][32];
    u64 ep[48], n1;
    u32 qv[128];
    u32 qcnt, q4, cnt[2], dec, pdone;
};

__device__ __forceinline__ void combine_publish(Smem& sm, int ab, int lane, rsrc_t rws, int h, int j, const Tag& tg) {
    const float mm = sm.ml[ab][lane & 15][0], ll = sm.ml[ab][lane & 15][1];
    const float M = trunc6(row16_max(mm));
    const float f = __builtin_amdgcn_exp2f(mm - M);
    const float L = row16_sum(f * ll);
    f32x2 o = {0.f, 0.f};
#pragma unroll
    for (int w = 0; w < NW; ++w) o += rdlane(f, w) * *(const f32x2*)&sm.part[ab][w][2 * lane];
    *(f32x2*)&sm.comb[ab][2 * lane] = o;
    lds_wait();
    __builtin_amdgcn_wave_barrier();
    if (lane < 48) {
        const int rh = lane >= 24, g = lane - rh * 24, g2 = g < 21 ? g : 21;
        const float* s = &sm.comb[ab][64 * rh + 3 * g2];
        const float v0 = s[0], v1 = g < 21 ? s[1] : M, v2 = g < 21 ? s[2] : L;
        st_sc1(rws, R_OFF + (((ab * 32 + h) * 2 + rh) * 8 + j) * 384 + g * 16, mkgran(v0, v1, v2, tg));
    }
}

__global__ void __launch_bounds__(1024) fused(const uint16_t* __restrict__ xA, const uint16_t* __restrict__ kA, const uint16_t* __restrict__ vA,
                                              const uint16_t* __restrict__ wqA, const uint16_t* __restrict__ woA,
                                              const uint16_t* __restrict__ xB, const uint16_t* __restrict__ kB, const uint16_t* __restrict__ vB,
                                              const uint16_t* __restrict__ wqB, const uint16_t* __restrict__ woB,
                                              unsigned char* __restrict__ ws, uint16_t* __restrict__ out) {
    __shared__ __attribute__((aligned(16))) Smem sm;
    const int b = blockIdx.x;
    const int h = b >> 3, j = b & 7;
    const int tid = threadIdx.x, lane = tid & 63, wave = __builtin_amdgcn_readfirstlane(tid >> 6), r = lane >> 4, c = lane & 15;
    const int row8 = lane >> 3, c8 = lane & 7;
    const int mat = j >> 2, rh = (j >> 1) & 1, ch = j & 1;
    const rsrc_t rws = mkrsrc(ws, WS_BYTES);
    const rsrc_t rkA = mkrsrc(kA, NH * SK * DH * 2), rvA = mkrsrc(vA, NH * SK * DH * 2);
    const rsrc_t rkB = mkrsrc(kB, NH * SK * DH * 2), rvB = mkrsrc(vB, NH * SK * DH * 2);
    const u32 lo = lane * 16;
    const u32 soB = (h * SK + (SK - WIN) + j * 256 + wave * 16) * 256;
    u32x4 buf[NL];
#define GX(i) ((u32)(((i) << 19) | (((i) & 1) << 14) | (((i) >> 1) << 12)))
#define LD(n) ((n) < 8 ? bld<2>(((n) & 4) ? rvB : rkB, lo + ((n) & 3) * 1024, soB) : (n) < 24 ? bld<2>(rkA, lo + ((n) & 3) * 1024, soK ^ GX(((n) - 8) >> 2)) : bld<2>(rvA, lo + ((n) & 3) * 1024, soV ^ GX(((n) - 24) >> 2)))
    __builtin_amdgcn_s_setprio(3);
    u32 soK = 0, soV = 0;
#define PRETOUCH()
    if (wave < 4) {
        const int hq = (h & ~1) | (j >> 2), qm = (j >> 1) & 1, qd0 = (j & 1) * 64 + (h & 1) * 32;
        const rsrc_t rw = mkrsrc(qm ? wqB : wqA, DM * HD * 2), rx = mkrsrc(qm ? xB : xA, DM * 2);
        const u32 wb = (wave * 160 * HD + hq * DH + qd0) * 2;
        const u32x4 x4 = bld<0>(rx, 320 * wave + 20 * (lane >> 2));
        const u32 x1 = __builtin_amdgcn_raw_buffer_load_b32(rx, 320 * wave + 20 * (lane >> 2) + 16, 0, 0);
        u32x4 w[10];
#pragma unroll
        for (int i = 0; i < 10; ++i) w[i] = bld<2>(rw, ((lane >> 2) * 10 * HD + (lane & 3) * 8) * 2, wb + i * HD * 2);
        PRETOUCH();
        __builtin_amdgcn_sched_barrier(0);
        lds_barrier();
        const u64 nq = sld64(ws + G_OFF + hq * 256) + 1;
        __builtin_amdgcn_sched_barrier(0);
        float q[8];
#pragma unroll
        for (int k = 0; k < 8; ++k) q[k] = 0.f;
#pragma unroll
        for (int i = 0; i < 10; ++i) {
            const u32 xw = i < 8 ? x4[i >> 1] : x1;
            const float xs = (i & 1) ? hi16(xw) : lo16(xw);
#pragma unroll
            for (int k = 0; k < 4; ++k) { q[2 * k] = fmaf(xs, lo16(w[i][k]), q[2 * k]); q[2 * k + 1] = fmaf(xs, hi16(w[i][k]), q[2 * k + 1]); }
        }
#pragma unroll
        for (int k = 0; k < 8; ++k) { q[k] += dpp<ROR4>(q[k]); q[k] += dpp<ROR8>(q[k]); q[k] = xrow_sum(q[k]); }
        if (lane < 4) { *(f32x4*)&sm.qp[wave][8 * lane] = f32x4{q[0], q[1], q[2], q[3]}; *(f32x4*)&sm.qp[wave][8 * lane + 4] = f32x4{q[4], q[5], q[6], q[7]}; }
        u32 old = 0;
        lds_wait();
        if (lane == 0) old = __hip_atomic_fetch_add(&sm.q4, 1u, __ATOMIC_RELAXED, __HIP_MEMORY_SCOPE_WORKGROUP);
        if (__builtin_amdgcn_readfirstlane(old) == 3) {
            asm volatile("" ::: "memory");
            const f32x4 s = (*(const f32x4*)&sm.qp[0][4 * c8] + *(const f32x4*)&sm.qp[1][4 * c8]) + (*(const f32x4*)&sm.qp[2][4 * c8] + *(const f32x4*)&sm.qp[3][4 * c8]);
            const u32 p0 = (__float_as_uint(bf16r(s[0])) >> 16) | (__float_as_uint(bf16r(s[1])) & 0xffff0000u);
            const u32 p1 = (__float_as_uint(bf16r(s[2])) >> 16) | (__float_as_uint(bf16r(s[3])) & 0xffff0000u);
            if (lane < 8) st_sc1(rws, Q_OFF + hq * 1024 + (qm * 128 + qd0) * 4 + lo, u32x4{p0, p1, (u32)(nq >> 32), (u32)nq});
        }
#pragma nounroll
        for (u32 spin = 0; spin < SPIN_MAX * 16; ++spin) {
            if (lds_ld(&sm.pdone)) break;
            __builtin_amdgcn_s_sleep(2);
        }
        asm volatile("" ::: "memory");
        {
            const u64 n1 = uni64(sm.n1);
            const u32 nhi = (u32)(n1 >> 32), nlo = (u32)n1;
            const u32 qb = __builtin_amdgcn_readfirstlane(Q_OFF + h * 1024 + wave * 256);
            bool ok = false;
            u32x16 d0, d1, d2, d3;
#pragma nounroll
            for (u32 spin = 0; spin < SPOLL; ++spin) {
                asm volatile("s_load_dwordx16 %0, %4, 0x0 glc\n\ts_load_dwordx16 %1, %4, 0x40 glc\n\ts_load_dwordx16 %2, %4, 0x80 glc\n\ts_load_dwordx16 %3, %4, 0xc0 glc\n\ts_waitcnt lgkmcnt(0)"
                             : "=&s"(d0), "=&s"(d1), "=&s"(d2), "=&s"(d3) : "s"(ws + qb) : "memory");
                if (((tbad(d0, nhi, nlo) | tbad(d1, nhi, nlo)) | (tbad(d2, nhi, nlo) | tbad(d3, nhi, nlo))) == 0) { ok = true; break; }
                __builtin_amdgcn_s_sleep(QSL);
            }
            u32 g0 = 0, g1 = 0;
            if (ok) {
#pragma unroll
                for (int i = 0; i < 4; ++i) {
                    g0 = wrlane(d0[4 * i], i, g0); g1 = wrlane(d0[4 * i + 1], i, g1);
                    g0 = wrlane(d1[4 * i], 4 + i, g0); g1 = wrlane(d1[4 * i + 1], 4 + i, g1);
                    g0 = wrlane(d2[4 * i], 8 + i, g0); g1 = wrlane(d2[4 * i + 1], 8 + i, g1);
                    g0 = wrlane(d3[4 * i], 12 + i, g0); g1 = wrlane(d3[4 * i + 1], 12 + i, g1);
                }
            } else {
                u32 off = qb + c * 16;
                u32x4 g;
#pragma nounroll
                for (u32 spin = 0; spin < SPIN_MAX; ++spin) {
                    asm volatile("" : "+v"(off));
                    g = ld_sc1(rws, off);
                    if (__all(((g[2] ^ nhi) | (g[3] ^ nlo)) == 0)) break;
                    __builtin_amdgcn_s_sleep(1);
                }
                g0 = g[0]; g1 = g[1];
            }
            if (lane < 16) *(u32x2*)&sm.qv[wave * 32 + 2 * lane] = u32x2{g0, g1};
            lds_wait();
            if (lane == 0) __hip_atomic_fetch_add(&sm.qcnt, 1u, __ATOMIC_RELAXED, __HIP_MEMORY_SCOPE_WORKGROUP);
        }
        __builtin_amdgcn_sched_barrier(0);
#pragma unroll
        for (int n = 0; n < 8; ++n) {
            if (n >= PW && lds_ld(&sm.dec) == 0) vm_wait<PW - 1>();
            buf[n] = LD(n);
            __builtin_amdgcn_sched_barrier(0);
        }
    } else if (wave == 12) {
        if (lane == 0) { sm.qcnt = 0; sm.q4 = 0; sm.cnt[0] = 0; sm.cnt[1] = 0; sm.dec = 0; sm.pdone = 0; }
        lds_barrier();
        int gs = 0;
        {
            const unsigned char* pa = (const unsigned char*)((j & 1) ? vA : kA) + (size_t)h * (SK * 256) + (j >> 1) * (16 * 32768);
#pragma unroll
            for (int i = 0; i < NPROBE; ++i) {
                const int side = ((j >> 1) ^ i) & 1;
                const unsigned char* p1 = pa + i * 32768 + side * 8192;
                const unsigned char* p2 = pa + i * 32768 + (side ^ 1) * 8192;
                u32 v1, v2; u64 t1, t2;
                asm volatile("s_load_dword %0, %4, 0x0 glc\n\ts_load_dword %1, %5, 0x0 glc\n\ts_waitcnt lgkmcnt(1)\n\ts_memtime %2\n\ts_waitcnt lgkmcnt(0)\n\ts_memtime %3\n\ts_waitcnt lgkmcnt(0)"
                             : "=&s"(v1), "=&s"(v2), "=&s"(t1), "=&s"(t2) : "s"(p1), "s"(p2) : "memory");
                const int g = min((int)(u32)(t2 - t1), PCAP);
                gs += side ? -g : g;
            }
        }
        const u64 n1 = sld64(ws + G_OFF + h * 256) + 1;
        const u32 nhi = (u32)(n1 >> 32), nlo = (u32)n1;
        const int iod0 = (__builtin_amdgcn_s_getreg((20 << 0) | (0 << 6) | (3 << 11)) >> 2) & 1;
        if (lane == 0) st_sc1(rws, D_OFF + h * 128 + j * 16, u32x4{(u32)(iod0 ? -gs : gs), (u32)iod0, nhi, nlo});
        if (lane == 0) { sm.n1 = n1; lds_wait(); __hip_atomic_store(&sm.pdone, 1u, __ATOMIC_RELAXED, __HIP_MEMORY_SCOPE_WORKGROUP); }
        int vg[8], vi[8];
        bool ok = false;
#pragma nounroll
        for (u32 spin = 0; spin < SPOLL; ++spin) {
            __builtin_amdgcn_s_sleep(2);
            u32x16 d0, d1;
            asm volatile("s_load_dwordx16 %0, %2, 0x0 glc\n\ts_load_dwordx16 %1, %2, 0x40 glc\n\ts_waitcnt lgkmcnt(0)" : "=&s"(d0), "=&s"(d1) : "s"(ws + (u32)__builtin_amdgcn_readfirstlane(D_OFF + h * 128)) : "memory");
#pragma unroll
            for (int i = 0; i < 4; ++i) { vg[i] = d0[4 * i]; vi[i] = d0[4 * i + 1]; vg[4 + i] = d1[4 * i]; vi[4 + i] = d1[4 * i + 1]; }
            if ((tbad(d0, nhi, nlo) | tbad(d1, nhi, nlo)) == 0) { ok = true; break; }
        }
        if (!ok) {
            u32 off = D_OFF + h * 128 + c8 * 16;
            u32x4 g;
#pragma nounroll
            for (u32 spin = 0; spin < SPIN_MAX; ++spin) {
                asm volatile("" : "+v"(off));
                g = ld_sc1(rws, off);
                if (__all(((g[2] ^ nhi) | (g[3] ^ nlo)) == 0)) break;
                __builtin_amdgcn_s_sleep(1);
            }
#pragma unroll
            for (int i = 0; i < 8; ++i) { vg[i] = __builtin_amdgcn_readlane(g[0], i); vi[i] = __builtin_amdgcn_readlane(g[1], i); }
        }
        int msk = 0;
#pragma unroll
        for (int i = 0; i < 8; ++i) msk |= (vi[i] & 1) << i;
        const int sk = (vg[0] + vg[2]) + (vg[4] + vg[6]), sv = (vg[1] + vg[3]) + (vg[5] + vg[7]);
        const int iod = (msk >> j) & 1;
        const int rank = __builtin_popcount((iod ? msk : ~msk) & ((1 << j) - 1));
        int pk_ = sk > 0 ? 0 : 1, pv_ = sv > 0 ? 0 : 1;
        const bool bal = __builtin_popcount(msk) == 4;
        const u32 d = 0x100u | (bal ? ((iod ^ pk_) * 4 + rank) | (((iod ^ pv_) * 4 + rank) << 4) : j * 17);
        if (lane == 0) __hip_atomic_store(&sm.dec, d, __ATOMIC_RELAXED, __HIP_MEMORY_SCOPE_WORKGROUP);
        PRETOUCH();
        __builtin_amdgcn_sched_barrier(0);
#pragma unroll
        for (int n = 0; n < 8; ++n) { buf[n] = LD(n); __builtin_amdgcn_sched_barrier(0); }
    } else {
        lds_barrier();
        if (QUIET) __builtin_amdgcn_s_sleep(QUIET);
        if (wave < 12) {
            const uint16_t* wsrc = (mat ? woB : woA) + (size_t)(h * DH + rh * 64 + row8) * DM + ch * 320 + c8 * 8;
#pragma unroll
            for (int i = 0; i < 5; ++i) {
                const int idx = (wave - 4) * 5 + i;
                if (i >= PW) vm_wait<PW - 1>();
                glds16(wsrc + (idx & 7) * 8 * DM + (idx >> 3) * 64, (u32)(uintptr_t)&sm.wo[idx * 64]);
            }
        }
        PRETOUCH();
        __builtin_amdgcn_sched_barrier(0);
        const u64 ev = __hip_atomic_load((const u64*)(ws + E_OFF) + (lane < 40 ? mat * 80 + ch * 40 + lane : b < NRED ? b : 0), __ATOMIC_RELAXED, __HIP_MEMORY_SCOPE_AGENT);
#pragma unroll
        for (int n = 0; n < 8; ++n) {
            if (lds_ld(&sm.dec) == 0) vm_wait<PW - 1>();
            buf[n] = LD(n);
            __builtin_amdgcn_sched_barrier(0);
        }
        if (wave == 8 && lane < 48) sm.ep[lane] = ev;
    }
    __builtin_amdgcn_sched_barrier(0);
    u32 dec = 0;
#pragma nounroll
    for (u32 spin = 0; spin < SPIN_MAX * 16; ++spin) {
        dec = lds_ld(&sm.dec);
        if (dec) break;
        __builtin_amdgcn_s_sleep(1);
    }
    const int kK = dec & 7, kV = (dec >> 4) & 7;
    const int lowb = (kK ^ (WMIX ? wave : 0)) & 3;
    soK = (u32)h * (SK * 256) + (wave << 15) + (((lowb & 1) << 14) | ((lowb & 2) << 11) | ((kK & 4) << 11));
    soV = (u32)h * (SK * 256) + (wave << 15) + (((lowb & 1) << 14) | ((lowb & 2) << 11) | ((kV & 4) << 11));
    __builtin_amdgcn_sched_barrier(0);
#pragma unroll
    for (int n = 8; n < PF; ++n) { buf[n] = LD(n); __builtin_amdgcn_sched_barrier(0); }
    __builtin_amdgcn_sched_barrier(0);
#pragma nounroll
    for (u32 spin = 0; spin < SPIN_MAX * 16; ++spin) {
        if (lds_ld(&sm.qcnt) == 4) break;
        __builtin_amdgcn_s_sleep(1);
    }
    asm volatile("" ::: "memory");
    const u32x4 qa4 = *(const u32x4*)&sm.qv[4 * c], qb4 = *(const u32x4*)&sm.qv[64 + 4 * c];
    const u64 n1 = uni64(sm.n1);
    const u32 nhi = (u32)(n1 >> 32), nlo = (u32)n1;
    const Tag tg = mktag(n1 - 1);
    float acc[8], m = -INFINITY, l = 0.f, sc[4];
#pragma unroll
    for (int k = 0; k < 8; ++k) acc[k] = 0.f;
#define FLUSH(AB) do { _Pragma("unroll") for (int k = 0; k < 8; ++k) acc[k] = xrow_sum(acc[k]); l = xrow_sum(l); \
        if (r == 0) { *(f32x4*)&sm.part[AB][wave][8 * c] = f32x4{acc[0], acc[1], acc[2], acc[3]}; *(f32x4*)&sm.part[AB][wave][8 * c + 4] = f32x4{acc[4], acc[5], acc[6], acc[7]}; \
                      if (c == 0) { sm.ml[AB][wave][0] = m; sm.ml[AB][wave][1] = l; } } } while (0)
#define NEXT(n) do { __builtin_amdgcn_sched_barrier(0); \
        if ((n) == FA1) __builtin_amdgcn_s_setprio(2); if ((n) == FA2) __builtin_amdgcn_s_setprio(1); if ((n) == FA3) __builtin_amdgcn_s_setprio(0); \
        if ((n) + PF >= 8 && (n) + PF < 24) buf[(n) + PF] = LD((n) + PF); \
        if ((n) + PFV >= 24 && (n) + PFV < NL) buf[(n) + PFV] = LD((n) + PFV); \
        __builtin_amdgcn_sched_barrier(0); } while (0)
#define SCORE(B, Q) ({ float d_ = dot2((B)[0], (Q)[0], 0.f); d_ = dot2((B)[1], (Q)[1], d_); d_ = dot2((B)[2], (Q)[2], d_); d_ = dot2((B)[3], (Q)[3], d_); \
                       d_ = row16_sum(d_); bf16r(bf16r(d_) * SCALE); })
#define PV(B, P) do { _Pragma("unroll") for (int k = 0; k < 4; ++k) { acc[2 * k] = fmaf((P), lo16((B)[k]), acc[2 * k]); acc[2 * k + 1] = fmaf((P), hi16((B)[k]), acc[2 * k + 1]); } \
        asm volatile("" : "+v"(acc[0]), "+v"(acc[1]), "+v"(acc[2]), "+v"(acc[3]), "+v"(acc[4]), "+v"(acc[5]), "+v"(acc[6]), "+v"(acc[7]) :: "memory"); } while (0)
#pragma unroll
    for (int n = 0; n < 4; ++n) { sc[n] = SCORE(buf[n], qb4) * LOG2E; asm volatile("" : "+v"(sc[n]) :: "memory"); NEXT(n); }
    m = xrow_max(fmaxf(fmaxf(sc[0], sc[1]), fmaxf(sc[2], sc[3])));
#pragma unroll
    for (int n = 4; n < 8; ++n) { const float p = __builtin_amdgcn_exp2f(sc[n & 3] - m); l += p; PV(buf[n], p); NEXT(n); }
    {
        FLUSH(1);
        u32 old = 0;
        lds_wait();
        if (lane == 0) old = __hip_atomic_fetch_add(&sm.cnt[1], 1u, __ATOMIC_RELAXED, __HIP_MEMORY_SCOPE_WORKGROUP);
        if (__builtin_amdgcn_readfirstlane(old) == NW - 1) { asm volatile("" ::: "memory"); combine_publish(sm, 1, lane, rws, h, j, tg); }
        l = 0.f;
#pragma unroll
        for (int k = 0; k < 8; ++k) acc[k] = 0.f;
        __builtin_amdgcn_sched_barrier(0);
    }
    const bool xch = kK != kV;
    u32 pk[8];
#pragma unroll
    for (int n = 8; n < 24; ++n) {
        const u32 sb = __float_as_uint(SCORE(buf[n], qa4));
        if (n & 1) pk[(n - 8) >> 1] |= sb & 0xffff0000u; else pk[(n - 8) >> 1] = sb >> 16;
        asm volatile("" : "+v"(pk[(n - 8) >> 1]) :: "memory");
        if (XDBG != 2 && (n & 3) == 3 && xch && c == 0) __builtin_amdgcn_raw_buffer_store_b128(u32x4{pk[((n - 8) >> 1) - 1], pk[(n - 8) >> 1], nhi, nlo}, rws, S_OFF + ((h * 8 + kK) * 16 + wave) * 256 + ((n - 8) >> 2) * 64 + r * 16, 0, SAUX);
        NEXT(n);
    }
    const u32 sb = __builtin_amdgcn_readfirstlane(S_OFF + ((h * 8 + kV) * 16 + wave) * 256);
    m = -INFINITY;
    u32 pend = 0;
    if (xch && XDBG != 1) {
        u32x16 d[4];
        asm volatile("s_load_dwordx16 %0, %4, 0x0 glc\n\ts_load_dwordx16 %1, %4, 0x40 glc\n\ts_load_dwordx16 %2, %4, 0x80 glc\n\ts_load_dwordx16 %3, %4, 0xc0 glc\n\ts_waitcnt lgkmcnt(0)"
                     : "=&s"(d[0]), "=&s"(d[1]), "=&s"(d[2]), "=&s"(d[3]) : "s"(ws + sb) : "memory");
#pragma unroll
        for (int i = 0; i < 4; ++i) {
            if (XDBG == 0) pend |= (tbad(d[i], nhi, nlo) != 0) << i;
#pragma unroll
            for (int k = 0; k < 4; ++k) { pk[2 * i] = wrlane(d[i][4 * k], 16 * k, pk[2 * i]); pk[2 * i + 1] = wrlane(d[i][4 * k + 1], 16 * k, pk[2 * i + 1]); }
        }
        asm volatile("s_nop 1" : "+v"(pk[6]), "+v"(pk[7]));
#pragma unroll
        for (int k = 0; k < 8; ++k) pk[k] = __builtin_amdgcn_mov_dpp(pk[k], 0x150, 0xf, 0xf, true);
    }
#pragma unroll
    for (int i = 0; i < 4; ++i) {
        u32 s0 = pk[2 * i], s1 = pk[2 * i + 1];
        if ((pend >> i) & 1) {
            bool ok = false;
            u32x16 d;
#pragma nounroll
            for (u32 spin = 0; spin < SPOLL; ++spin) {
                d = sld16(ws + sb + i * 64);
                if (tbad(d, nhi, nlo) == 0) { ok = true; break; }
                __builtin_amdgcn_s_sleep(SSL);
            }
            if (ok) {
#pragma unroll
                for (int k = 0; k < 4; ++k) { s0 = wrlane(d[4 * k], 16 * k, s0); s1 = wrlane(d[4 * k + 1], 16 * k, s1); }
            } else {
                u32 soff = sb + i * 64 + r * 16;
                u32x4 g;
#pragma nounroll
                for (u32 spin = 0; spin < SPIN_MAX; ++spin) {
                    asm volatile("" : "+v"(soff));
                    g = ld_sc1(rws, soff);
                    if (__all(((g[2] ^ nhi) | (g[3] ^ nlo)) == 0)) break;
                    __builtin_amdgcn_s_sleep(1);
                }
                s0 = g[0]; s1 = g[1];
            }
            asm volatile("s_nop 1" : "+v"(s0), "+v"(s1));
            s0 = __builtin_amdgcn_mov_dpp(s0, 0x150, 0xf, 0xf, true); s1 = __builtin_amdgcn_mov_dpp(s1, 0x150, 0xf, 0xf, true);
        }
        const float mn = fmaxf(m, xrow_max(fmaxf(fmaxf(lo16(s0), hi16(s0)), fmaxf(lo16(s1), hi16(s1)))) * LOG2E);
        if (i) {
            const float f = __builtin_amdgcn_exp2f(m - mn);
            l *= f;
#pragma unroll
            for (int k = 0; k < 8; ++k) acc[k] *= f;
        }
        m = mn;
        __builtin_amdgcn_sched_barrier(0);
#pragma unroll
        for (int n = 24 + 4 * i; n < 28 + 4 * i; ++n) {
            const u32 sw = (n & 2) ? s1 : s0;
            const float p = __builtin_amdgcn_exp2f(((n & 1) ? hi16(sw) : lo16(sw)) * LOG2E - m);
            l += p; PV(buf[n], p); NEXT(n);
        }
    }
    FLUSH(0);
    {
        u32 old = 0;
        asm volatile("s_waitcnt vmcnt(0) lgkmcnt(0)" ::: "memory");
        if (lane == 0) old = __hip_atomic_fetch_add(&sm.cnt[0], 1u, __ATOMIC_RELAXED, __HIP_MEMORY_SCOPE_WORKGROUP);
        if (__builtin_amdgcn_readfirstlane(old) == NW - 1) { asm volatile("" ::: "memory"); __builtin_amdgcn_s_setprio(3); combine_publish(sm, 0, lane, rws, h, kV, tg); }
    }
    if (wave == 5) {
        __builtin_amdgcn_s_setprio(3);
        u32 off = R_OFF + ((mat * 32 + h) * 2 + rh) * 3072 + lo;
        u32x4 g[3];
#pragma nounroll
        for (u32 spin = 0; spin < SPIN_MAX; ++spin) {
            asm volatile("" : "+v"(off));
#pragma unroll
            for (int i = 0; i < 3; ++i) g[i] = ld_sc1(rws, off + i * 1024);
            if (__all((gbad(g[0], tg) | gbad(g[1], tg) | gbad(g[2], tg)) == 0)) break;
            __builtin_amdgcn_s_sleep(GSLEEP);
        }
#pragma unroll
        for (int i = 0; i < 3; ++i) { float* d = &sm.tsl[3 * (lane + 64 * i)]; d[0] = gval(g[i][0]); d[1] = gval(g[i][1]); d[2] = gval(g[i][2]); }
        lds_wait();
        __builtin_amdgcn_wave_barrier();
        float M = sm.tsl[64];
#pragma unroll
        for (int p = 1; p < 8; ++p) M = fmaxf(M, sm.tsl[72 * p + 64]);
        float o = 0.f, L = 0.f;
#pragma unroll
        for (int p = 0; p < 8; ++p) {
            const float f = __builtin_amdgcn_exp2f(sm.tsl[72 * p + 64] - M);
            o = fmaf(f, sm.tsl[72 * p + lane], o); L = fmaf(f, sm.tsl[72 * p + 65], L);
        }
        sm.ov[lane] = bf16r(o / L);
    }
    lds_barrier();
    if (wave < 5) {
        const Tag ty = mktag(sm.ep[wave * 8 + c8]);
        float y[8];
#pragma unroll
        for (int k = 0; k < 8; ++k) y[k] = 0.f;
#pragma unroll
        for (int i = 0; i < 8; ++i) {
            const float sv = sm.ov[row8 + 8 * i];
            const u32x4 wv = sm.wo[(wave * 8 + i) * 64 + lane];
#pragma unroll
            for (int k = 0; k < 4; ++k) { y[2 * k] = fmaf(sv, lo16(wv[k]), y[2 * k]); y[2 * k + 1] = fmaf(sv, hi16(wv[k]), y[2 * k + 1]); }
        }
#pragma unroll
        for (int k = 0; k < 8; ++k) { y[k] += dpp<ROR8>(y[k]); y[k] = xrow_sum(y[k]); }
        if (lane < 32) st_sc1(rws, Y_OFF + ((mat * 80 + ch * 40 + wave * 8 + c8) * 64 + h * 2 + rh) * 64 + row8 * 16, GRAN8(y, row8, ty));
    } else if (wave == 5 && b < NRED) {
        const u64 evb = uni64(sm.ep[40]);
        const Tag tr = mktag(evb);
        u32 off = Y_OFF + b * 4096 + lo;
        u32x4 g[4];
#pragma nounroll
        for (u32 spin = 0; spin < SPIN_MAX; ++spin) {
            asm volatile("" : "+v"(off));
#pragma unroll
            for (int i = 0; i < 4; ++i) g[i] = ld_sc1(rws, off + i * 1024);
            if (__all((gbad(g[0], tr) | gbad(g[1], tr) | gbad(g[2], tr) | gbad(g[3], tr)) == 0)) break;
            __builtin_amdgcn_s_sleep(1);
        }
#pragma unroll
        for (int k = 0; k < 3; ++k) {
            float t = (gval(g[0][k]) + gval(g[1][k])) + (gval(g[2][k]) + gval(g[3][k]));
            t += dpp<ROR4>(t); t += dpp<ROR8>(t); t = xrow_sum(t);
            if (lane < (k < 2 ? 3 : 2)) out[b * 8 + 3 * lane + k] = (uint16_t)(__float_as_uint(bf16r(t)) >> 16);
        }
        if (lane == 0) __hip_atomic_store((u64*)(ws + E_OFF) + b, evb + 1, __ATOMIC_RELAXED, __HIP_MEMORY_SCOPE_AGENT);
    } else if (wave == 6 && j == 0 && lane == 0) __hip_atomic_store((u64*)(ws + G_OFF + h * 256), n1, __ATOMIC_RELAXED, __HIP_MEMORY_SCOPE_AGENT);
}

extern "C" void kernel_launch(void* const* d_in, const int* in_sizes, int n_in,
                              void* d_out, int out_size, void* d_ws, size_t ws_size,
                              hipStream_t stream) {
    fused<<<NWG, 1024, 0, stream>>>((const uint16_t*)d_in[0], (const uint16_t*)d_in[1], (const uint16_t*)d_in[2], (const uint16_t*)d_in[3], (const uint16_t*)d_in[4],
                                    (const uint16_t*)d_in[5], (const uint16_t*)d_in[6], (const uint16_t*)d_in[7], (const uint16_t*)d_in[8], (const uint16_t*)d_in[9],
                                    (unsigned char*)d_ws, (uint16_t*)d_out);
}
```

```cpp
#include <hip/hip_runtime.h>
#include <hip/hip_bf16.h>
#include <stdint.h>

typedef unsigned int u32;
typedef unsigned long long u64;
typedef __attribute__((ext_vector_type(4))) unsigned int u32x4;
typedef __attribute__((ext_vector_type(2))) unsigned int u32x2;
typedef __attribute__((ext_vector_type(16))) unsigned int u32x16;
typedef __attribute__((ext_vector_type(4))) float f32x4;
typedef __attribute__((ext_vector_type(2))) float f32x2;
typedef __attribute__((ext_vector_type(2))) __bf16 bf16x2;
typedef __attribute__((ext_vector_type(8))) __bf16 bf16x8;
typedef __amdgpu_buffer_rsrc_t rsrc_t;

constexpr int NH = 32, SK = 8192, DH = 128, DM = 640, HD = NH * DH;
constexpr float SCALE = 0.08838834764831845f;
constexpr float LOG2E = 1.4426950408889634f;
constexpr int NWG = 256, NW = 16;
constexpr int NL = 40;
constexpr int NRED = 160;
#define PF 8
#define PFV 6
#define FA1 0
#define FA2 12
#define FA3 24
#define PW 2
#define NPROBE 2
#define GSL2 8
#define DSL 4
#define PCAP 200
#define GSLEEP 1
#define RSLEEP 1
#define QSL 4
#define SSL 4
constexpr u32 SPOLL = 64;
constexpr u32 SPIN_MAX = 1u << 20;
constexpr u32 E_OFF = 0;
constexpr u32 G_OFF = 2048;
constexpr u32 Q_OFF = 16384;
constexpr u32 R_OFF = Q_OFF + 32 * 1024;
constexpr u32 Y_OFF = R_OFF + 2 * 32 * 2 * 4096;
constexpr u32 D_OFF = Y_OFF + NRED * 64 * 64;
constexpr u32 S_OFF = D_OFF + 32 * 128;
constexpr u32 WS_BYTES = S_OFF + 32 * 8 * 16 * 256;

__device__ __forceinline__ float dot2(u32 a, u32 b, float c) {
    return __builtin_amdgcn_fdot2_f32_bf16(__builtin_bit_cast(bf16x2, a), __builtin_bit_cast(bf16x2, b), c, false);
}
template <int CTRL> __device__ __forceinline__ float dpp(float x) {
    return __builtin_bit_cast(float, __builtin_amdgcn_mov_dpp(__builtin_bit_cast(int, x), CTRL, 0xf, 0xf, true));
}
constexpr int XOR1 = 0xB1, XOR2 = 0x4E, HMIR = 0x141, MIR = 0x140, ROR4 = 0x124, ROR8 = 0x128;
__device__ __forceinline__ float row16_sum(float x) {
    x += dpp<XOR1>(x); x += dpp<XOR2>(x); x += dpp<HMIR>(x); x += dpp<MIR>(x); return x;
}
__device__ __forceinline__ float row16_max(float x) {
    x = fmaxf(x, dpp<XOR1>(x)); x = fmaxf(x, dpp<XOR2>(x)); x = fmaxf(x, dpp<HMIR>(x)); x = fmaxf(x, dpp<MIR>(x)); return x;
}
__device__ __forceinline__ float sw16_sum2(float a, float b) {
    auto s = __builtin_amdgcn_permlane16_swap(__float_as_uint(a), __float_as_uint(b), false, false);
    return __uint_as_float(s[0]) + __uint_as_float(s[1]);
}
__device__ __forceinline__ float sw32_sum2(float a, float b) {
    auto t = __builtin_amdgcn_permlane32_swap(__float_as_uint(a), __float_as_uint(b), false, false);
    return __uint_as_float(t[0]) + __uint_as_float(t[1]);
}
__device__ __forceinline__ float xrow_sum(float x) { x = sw16_sum2(x, x); return sw32_sum2(x, x); }
__device__ __forceinline__ float sw32_max(float x) {
    auto t = __builtin_amdgcn_permlane32_swap(__float_as_uint(x), __float_as_uint(x), false, false);
    return fmaxf(__uint_as_float(t[0]), __uint_as_float(t[1]));
}
__device__ __forceinline__ float xrow_max(float x) {
    auto s = __builtin_amdgcn_permlane16_swap(__float_as_uint(x), __float_as_uint(x), false, false);
    return sw32_max(fmaxf(__uint_as_float(s[0]), __uint_as_float(s[1])));
}
__device__ __forceinline__ float rdlane(float x, int l) { return __builtin_bit_cast(float, __builtin_amdgcn_readlane(__builtin_bit_cast(int, x), l)); }
__device__ __forceinline__ float bf16r(float x) { return (float)(__bf16)x; }
__device__ __forceinline__ float lo16(u32 w) { return __uint_as_float(w << 16); }
__device__ __forceinline__ float hi16(u32 w) { return __uint_as_float(w & 0xffff0000u); }
__device__ __forceinline__ rsrc_t mkrsrc(const void* p, u32 bytes) { return __builtin_amdgcn_make_buffer_rsrc((void*)p, 0, bytes, 0x00020000); }
template <int AUX> __device__ __forceinline__ u32x4 bld(rsrc_t r, u32 off, u32 soff = 0) { return __builtin_amdgcn_raw_buffer_load_b128(r, off, soff, AUX); }
__device__ __forceinline__ u32x4 ld_sc1(rsrc_t r, u32 off, u32 soff = 0) { return __builtin_amdgcn_raw_buffer_load_b128(r, off, soff, 16); }
__device__ __forceinline__ void st_sc1(rsrc_t r, u32 off, u32x4 v) { __builtin_amdgcn_raw_buffer_store_b128(v, r, off, 0, 16); }
__device__ __forceinline__ void lds_barrier() { asm volatile("s_waitcnt lgkmcnt(0)\n\ts_barrier" ::: "memory"); }
__device__ __forceinline__ void lds_wait() { asm volatile("s_waitcnt lgkmcnt(0)" ::: "memory"); }
template <int N> __device__ __forceinline__ void vm_wait() { __builtin_amdgcn_s_waitcnt(0x0f70 | (N & 15) | ((N >> 4) << 14)); }
__device__ __forceinline__ u64 uni64(u64 v) { return ((u64)(u32)__builtin_amdgcn_readfirstlane((u32)(v >> 32)) << 32) | (u32)__builtin_amdgcn_readfirstlane((u32)v); }

__device__ __forceinline__ u64 sld64(const void* p) { u64 v; asm volatile("s_load_dwordx2 %0, %1, 0x0 glc\n\ts_waitcnt lgkmcnt(0)" : "=&s"(v) : "s"(p) : "memory"); return v; }
__device__ __forceinline__ u32x16 sld16(const void* p) { u32x16 v; asm volatile("s_load_dwordx16 %0, %1, 0x0 glc\n\ts_waitcnt lgkmcnt(0)" : "=&s"(v) : "s"(p) : "memory"); return v; }
__device__ __forceinline__ u32 tbad(const u32x16& d, u32 hi, u32 lo) {
    return ((d[2] ^ hi) | (d[3] ^ lo)) | ((d[6] ^ hi) | (d[7] ^ lo)) | ((d[10] ^ hi) | (d[11] ^ lo)) | ((d[14] ^ hi) | (d[15] ^ lo));
}
__device__ __forceinline__ u32 wrlane(u32 v, int l, u32 old) { asm("v_writelane_b32 %0, %1, %2" : "+v"(old) : "s"(v), "n"(l)); return old; }
__device__ __forceinline__ void glds16(const void* gsrc, u32 lds_dst) {
    u32 keep;
    asm volatile("s_mov_b32 %0, m0\n\ts_mov_b32 m0, %2\n\ts_nop 0\n\tglobal_load_lds_dwordx4 %1, off nt\n\ts_mov_b32 m0, %0" : "=&s"(keep) : "v"(gsrc), "s"(lds_dst) : "memory");
}
__device__ __forceinline__ u32 lds_ld(const u32* p) { return __builtin_amdgcn_readfirstlane(__hip_atomic_load(p, __ATOMIC_RELAXED, __HIP_MEMORY_SCOPE_WORKGROUP)); }
__device__ __forceinline__ u32 bad48(u32x4 g, u32 hi, u32 lo) { return (((g[0] ^ hi) | (g[1] ^ (hi >> 8))) & 255u) | (g[3] ^ lo); }
__device__ __forceinline__ u32 bad64(u32x4 g, u32 hi, u32 lo) { return (g[2] ^ hi) | (g[3] ^ lo); }

struct Smem {
    u32x4 wo[40 * 64];
    float part[2][NW][128];
    float ml[2][NW][2];
    u32x4 ov[8];
    float qp[4][32];
    u64 ep[48], n1;
    u32 qv[128];
    u32 qcnt, q4, cnt[2], dec, pdone;
};

__device__ __forceinline__ void combine_publish(Smem& sm, int ab, int lane, rsrc_t rws, int h, int slot, u32 nhi, u32 nlo) {
    const f32x2 ml = *(const f32x2*)&sm.ml[ab][lane & 15][0];
    f32x2 pv[NW];
#pragma unroll
    for (int w = 0; w < NW; ++w) pv[w] = *(const f32x2*)&sm.part[ab][w][2 * lane];
    const u32 off = R_OFF + (((ab * 32 + h) * 2 + (lane >> 5)) * 8 + slot) * 512 + (lane & 31) * 16;
    __builtin_amdgcn_sched_barrier(0);
    const float M = row16_max(ml[0]);
    const float f = __builtin_amdgcn_exp2f(ml[0] - M);
    const float L = row16_sum(f * ml[1]);
    f32x2 o[4];
#pragma unroll
    for (int w = 0; w < NW; ++w) { if (w < 4) o[w] = rdlane(f, w) * pv[w]; else o[w & 3] += rdlane(f, w) * pv[w]; }
    const f32x2 t = ((o[0] + o[1]) + (o[2] + o[3])) * __builtin_amdgcn_rcpf(L);
    const float W = M + __builtin_amdgcn_logf(L);
    st_sc1(rws, off, u32x4{(__float_as_uint(t[0]) & ~255u) | (nhi & 255u), (__float_as_uint(t[1]) & ~255u) | ((nhi >> 8) & 255u), __float_as_uint(W), nlo});
}

__global__ void __launch_bounds__(1024) fused(const uint16_t* __restrict__ xA, const uint16_t* __restrict__ kA, const uint16_t* __restrict__ vA,
                                              const uint16_t* __restrict__ wqA, const uint16_t* __restrict__ woA,
                                              const uint16_t* __restrict__ xB, const uint16_t* __restrict__ kB, const uint16_t* __restrict__ vB,
                                              const uint16_t* __restrict__ wqB, const uint16_t* __restrict__ woB,
                                              unsigned char* __restrict__ ws, uint16_t* __restrict__ out) {
    __shared__ __attribute__((aligned(16))) Smem sm;
    const int b = blockIdx.x;
    const int h = b >> 3, j = b & 7;
    const int tid = threadIdx.x, lane = tid & 63, wave = __builtin_amdgcn_readfirstlane(tid >> 6), r = lane >> 4, c = lane & 15;
    const int row8 = lane >> 3, c8 = lane & 7;
    const int mat = j >> 2, rh = (j >> 1) & 1, ch = j & 1;
    const rsrc_t rws = mkrsrc(ws, WS_BYTES);
    const rsrc_t rkA = mkrsrc(kA, NH * SK * DH * 2), rvA = mkrsrc(vA, NH * SK * DH * 2);
    const rsrc_t rkB = mkrsrc(kB, NH * SK * DH * 2), rvB = mkrsrc(vB, NH * SK * DH * 2);
    const u32 lo = lane * 16;
#define CLS(k) ((u32)((((k) & 1) << 14) | (((k) & 2) << 11) | (((k) & 4) << 11)))
    const u32 soB = (u32)h * (SK * 256) + ((48 + wave) << 15) + CLS(j ^ (wave & 7));
    u32x4 buf[NL];
#define GX(i) ((u32)(((i) << 19) | (((i) & 1) << 14) | (((i) >> 1) << 12)))
#define LD(n) ((n) < 8 ? bld<2>(((n) & 4) ? rvB : rkB, lo + ((n) & 3) * 1024, soB) : (n) < 24 ? bld<2>(rkA, lo + ((n) & 3) * 1024, soK ^ GX(((n) - 8) >> 2)) : bld<2>(rvA, lo + ((n) & 3) * 1024, soV ^ GX(((n) - 24) >> 2)))
    __builtin_amdgcn_s_setprio(3);
    u32 soK = 0, soV = 0;
    if (wave < 4) {
        const int hq = (h & ~1) | (j >> 2), qm = (j >> 1) & 1, qd0 = (j & 1) * 64 + (h & 1) * 32;
        const rsrc_t rw = mkrsrc(qm ? wqB : wqA, DM * HD * 2), rx = mkrsrc(qm ? xB : xA, DM * 2);
        const u32 wb = (wave * 160 * HD + hq * DH + qd0) * 2;
        const u32x4 x4 = bld<0>(rx, 320 * wave + 20 * (lane >> 2));
        const u32 x1 = __builtin_amdgcn_raw_buffer_load_b32(rx, 320 * wave + 20 * (lane >> 2) + 16, 0, 0);
        u32x4 w[10];
#pragma unroll
        for (int i = 0; i < 10; ++i) w[i] = bld<2>(rw, ((lane >> 2) * 10 * HD + (lane & 3) * 8) * 2, wb + i * HD * 2);
        __builtin_amdgcn_sched_barrier(0);
        lds_barrier();
        const u64 nq = sld64(ws + G_OFF + hq * 256) + 1;
        __builtin_amdgcn_sched_barrier(0);
        float q[8];
#pragma unroll
        for (int k = 0; k < 8; ++k) q[k] = 0.f;
#pragma unroll
        for (int i = 0; i < 10; ++i) {
            const u32 xw = i < 8 ? x4[i >> 1] : x1;
            const float xs = (i & 1) ? hi16(xw) : lo16(xw);
#pragma unroll
            for (int k = 0; k < 4; ++k) { q[2 * k] = fmaf(xs, lo16(w[i][k]), q[2 * k]); q[2 * k + 1] = fmaf(xs, hi16(w[i][k]), q[2 * k + 1]); }
        }
#pragma unroll
        for (int k = 0; k < 8; ++k) { q[k] += dpp<ROR4>(q[k]); q[k] += dpp<ROR8>(q[k]); q[k] = xrow_sum(q[k]); }
        if (lane < 4) { *(f32x4*)&sm.qp[wave][8 * lane] = f32x4{q[0], q[1], q[2], q[3]}; *(f32x4*)&sm.qp[wave][8 * lane + 4] = f32x4{q[4], q[5], q[6], q[7]}; }
        u32 old = 0;
        lds_wait();
        if (lane == 0) old = __hip_atomic_fetch_add(&sm.q4, 1u, __ATOMIC_RELAXED, __HIP_MEMORY_SCOPE_WORKGROUP);
        if (__builtin_amdgcn_readfirstlane(old) == 3) {
            asm volatile("" ::: "memory");
            const f32x4 s = (*(const f32x4*)&sm.qp[0][4 * c8] + *(const f32x4*)&sm.qp[1][4 * c8]) + (*(const f32x4*)&sm.qp[2][4 * c8] + *(const f32x4*)&sm.qp[3][4 * c8]);
            const u32 p0 = (__float_as_uint(bf16r(s[0])) >> 16) | (__float_as_uint(bf16r(s[1])) & 0xffff0000u);
            const u32 p1 = (__float_as_uint(bf16r(s[2])) >> 16) | (__float_as_uint(bf16r(s[3])) & 0xffff0000u);
            if (lane < 8) st_sc1(rws, Q_OFF + hq * 1024 + (qm * 128 + qd0) * 4 + lo, u32x4{p0, p1, (u32)(nq >> 32), (u32)nq});
        }
#pragma nounroll
        for (u32 spin = 0; spin < SPIN_MAX * 16; ++spin) {
            if (lds_ld(&sm.pdone)) break;
            __builtin_amdgcn_s_sleep(2);
        }
        asm volatile("" ::: "memory");
        {
            const u64 n1 = uni64(sm.n1);
            const u32 nhi = (u32)(n1 >> 32), nlo = (u32)n1;
            const u32 qb = __builtin_amdgcn_readfirstlane(Q_OFF + h * 1024 + wave * 256);
            bool ok = false;
            u32x16 d0, d1, d2, d3;
#pragma nounroll
            for (u32 spin = 0; spin < SPOLL; ++spin) {
                asm volatile("s_load_dwordx16 %0, %4, 0x0 glc\n\ts_load_dwordx16 %1, %4, 0x40 glc\n\ts_load_dwordx16 %2, %4, 0x80 glc\n\ts_load_dwordx16 %3, %4, 0xc0 glc\n\ts_waitcnt lgkmcnt(0)"
                             : "=&s"(d0), "=&s"(d1), "=&s"(d2), "=&s"(d3) : "s"(ws + qb) : "memory");
                if (((tbad(d0, nhi, nlo) | tbad(d1, nhi, nlo)) | (tbad(d2, nhi, nlo) | tbad(d3, nhi, nlo))) == 0) { ok = true; break; }
                __builtin_amdgcn_s_sleep(QSL);
            }
            u32 g0 = 0, g1 = 0;
            if (ok) {
#pragma unroll
                for (int i = 0; i < 4; ++i) {
                    g0 = wrlane(d0[4 * i], i, g0); g1 = wrlane(d0[4 * i + 1], i, g1);
                    g0 = wrlane(d1[4 * i], 4 + i, g0); g1 = wrlane(d1[4 * i + 1], 4 + i, g1);
                    g0 = wrlane(d2[4 * i], 8 + i, g0); g1 = wrlane(d2[4 * i + 1], 8 + i, g1);
                    g0 = wrlane(d3[4 * i], 12 + i, g0); g1 = wrlane(d3[4 * i + 1], 12 + i, g1);
                }
            } else {
                u32 off = qb + c * 16;
                u32x4 g;
#pragma nounroll
                for (u32 spin = 0; spin < SPIN_MAX; ++spin) {
                    asm volatile("" : "+v"(off));
                    g = ld_sc1(rws, off);
                    if (__all(bad64(g, nhi, nlo) == 0)) break;
                    __builtin_amdgcn_s_sleep(1);
                }
                g0 = g[0]; g1 = g[1];
            }
            if (lane < 16) *(u32x2*)&sm.qv[wave * 32 + 2 * lane] = u32x2{g0, g1};
            lds_wait();
            if (lane == 0) __hip_atomic_fetch_add(&sm.qcnt, 1u, __ATOMIC_RELAXED, __HIP_MEMORY_SCOPE_WORKGROUP);
        }
        __builtin_amdgcn_sched_barrier(0);
#pragma unroll
        for (int n = 0; n < 8; ++n) {
            if (n >= PW && lds_ld(&sm.dec) == 0) vm_wait<PW - 1>();
            buf[n] = LD(n);
            __builtin_amdgcn_sched_barrier(0);
        }
    } else if (wave == 12) {
        if (lane == 0) { sm.qcnt = 0; sm.q4 = 0; sm.cnt[0] = 0; sm.cnt[1] = 0; sm.dec = 0; sm.pdone = 0; }
        lds_barrier();
        int gs = 0;
        {
            const unsigned char* pa = (const unsigned char*)((j & 1) ? vA : kA) + (size_t)h * (SK * 256) + (j >> 1) * (16 * 32768);
#pragma unroll
            for (int i = 0; i < NPROBE; ++i) {
                const int side = ((j >> 1) ^ i) & 1;
                const unsigned char* p1 = pa + i * 32768 + side * 8192;
                const unsigned char* p2 = pa + i * 32768 + (side ^ 1) * 8192;
                u32 v1, v2; u64 t1, t2;
                asm volatile("s_load_dword %0, %4, 0x0 glc\n\ts_load_dword %1, %5, 0x0 glc\n\ts_waitcnt lgkmcnt(1)\n\ts_memtime %2\n\ts_waitcnt lgkmcnt(0)\n\ts_memtime %3\n\ts_waitcnt lgkmcnt(0)"
                             : "=&s"(v1), "=&s"(v2), "=&s"(t1), "=&s"(t2) : "s"(p1), "s"(p2) : "memory");
                const int g = min((int)(u32)(t2 - t1), PCAP);
                gs += side ? -g : g;
            }
        }
        u64 e1;
        {
            u32 dummy;
            asm volatile("s_load_dword %1, %3, 0x0\n\ts_load_dwordx2 %0, %2, 0x0 glc\n\ts_waitcnt lgkmcnt(0)" : "=&s"(e1), "=&s"(dummy)
                         : "s"(ws + G_OFF + h * 256), "s"((const unsigned char*)((j & 1) ? kA : vA) + (size_t)h * (SK * 256) + j * 4096) : "memory");
        }
        const u64 n1 = e1 + 1;
        const u32 nhi = (u32)(n1 >> 32), nlo = (u32)n1;
        const int iod0 = (__builtin_amdgcn_s_getreg((20 << 0) | (0 << 6) | (3 << 11)) >> 2) & 1;
        if (lane == 0) st_sc1(rws, D_OFF + h * 128 + j * 16, u32x4{(u32)(iod0 ? -gs : gs), (u32)iod0, nhi, nlo});
        if (lane == 0) { sm.n1 = n1; lds_wait(); __hip_atomic_store(&sm.pdone, 1u, __ATOMIC_RELAXED, __HIP_MEMORY_SCOPE_WORKGROUP); }
        int vg[8], vi[8];
        bool ok = false;
#pragma nounroll
        for (u32 spin = 0; spin < SPOLL; ++spin) {
            __builtin_amdgcn_s_sleep(2);
            u32x16 d0, d1;
            asm volatile("s_load_dwordx16 %0, %2, 0x0 glc\n\ts_load_dwordx16 %1, %2, 0x40 glc\n\ts_waitcnt lgkmcnt(0)" : "=&s"(d0), "=&s"(d1) : "s"(ws + (u32)__builtin_amdgcn_readfirstlane(D_OFF + h * 128)) : "memory");
#pragma unroll
            for (int i = 0; i < 4; ++i) { vg[i] = d0[4 * i]; vi[i] = d0[4 * i + 1]; vg[4 + i] = d1[4 * i]; vi[4 + i] = d1[4 * i + 1]; }
            if ((tbad(d0, nhi, nlo) | tbad(d1, nhi, nlo)) == 0) { ok = true; break; }
        }
        if (!ok) {
            u32 off = D_OFF + h * 128 + c8 * 16;
            u32x4 g;
#pragma nounroll
            for (u32 spin = 0; spin < SPIN_MAX; ++spin) {
                asm volatile("" : "+v"(off));
                g = ld_sc1(rws, off);
                if (__all(bad64(g, nhi, nlo) == 0)) break;
                __builtin_amdgcn_s_sleep(1);
            }
#pragma unroll
            for (int i = 0; i < 8; ++i) { vg[i] = __builtin_amdgcn_readlane(g[0], i); vi[i] = __builtin_amdgcn_readlane(g[1], i); }
        }
        int msk = 0;
#pragma unroll
        for (int i = 0; i < 8; ++i) msk |= (vi[i] & 1) << i;
        const int sk = (vg[0] + vg[2]) + (vg[4] + vg[6]), sv = (vg[1] + vg[3]) + (vg[5] + vg[7]);
        const int iod = (msk >> j) & 1;
        const int rank = __builtin_popcount((iod ? msk : ~msk) & ((1 << j) - 1));
        const int pk_ = sk > 0 ? 0 : 1, pv_ = sv > 0 ? 0 : 1;
        const bool bal = __builtin_popcount(msk) == 4;
        const u32 d = 0x100u | (bal ? ((iod ^ pk_) * 4 + rank) | (((iod ^ pv_) * 4 + rank) << 4) : j * 17);
        if (lane == 0) __hip_atomic_store(&sm.dec, d, __ATOMIC_RELAXED, __HIP_MEMORY_SCOPE_WORKGROUP);
        __builtin_amdgcn_sched_barrier(0);
#pragma unroll
        for (int n = 0; n < 8; ++n) { buf[n] = LD(n); __builtin_amdgcn_sched_barrier(0); }
    } else {
        lds_barrier();
        if (wave < 12) {
            const uint16_t* wsrc = (mat ? woB : woA) + (size_t)(h * DH + rh * 64 + row8) * DM + ch * 320 + c8 * 8;
#pragma unroll
            for (int i = 0; i < 5; ++i) {
                const int idx = (wave - 4) * 5 + i;
                if (i >= PW) vm_wait<PW - 1>();
                glds16(wsrc + (idx & 7) * 8 * DM + (idx >> 3) * 64, (u32)(uintptr_t)&sm.wo[idx * 64]);
            }
        }
        __builtin_amdgcn_sched_barrier(0);
        const u64 ev = __hip_atomic_load((const u64*)(ws + E_OFF) + (lane < 40 ? mat * 80 + ch * 40 + lane : b < NRED ? b : 0), __ATOMIC_RELAXED, __HIP_MEMORY_SCOPE_AGENT);
#pragma unroll
        for (int n = 0; n < 8; ++n) {
            if (lds_ld(&sm.dec) == 0) vm_wait<PW - 1>();
            buf[n] = LD(n);
            __builtin_amdgcn_sched_barrier(0);
        }
        if (wave == 8 && lane < 48) sm.ep[lane] = ev + 1;
    }
    __builtin_amdgcn_sched_barrier(0);
    u32 dec = 0;
#pragma nounroll
    for (u32 spin = 0; spin < SPIN_MAX * 16; ++spin) {
        dec = lds_ld(&sm.dec);
        if (dec) break;
        __builtin_amdgcn_s_sleep(DSL);
    }
    const int kK = dec & 7, kV = (dec >> 4) & 7;
    soK = (u32)h * (SK * 256) + (wave << 15) + CLS(kK);
    soV = (u32)h * (SK * 256) + (wave << 15) + CLS(kV);
    __builtin_amdgcn_sched_barrier(0);
#pragma unroll
    for (int n = 8; n < 8 + PF; ++n) { buf[n] = LD(n); __builtin_amdgcn_sched_barrier(0); }
    __builtin_amdgcn_sched_barrier(0);
#pragma nounroll
    for (u32 spin = 0; spin < SPIN_MAX * 16; ++spin) {
        if (lds_ld(&sm.qcnt) == 4) break;
        __builtin_amdgcn_s_sleep(DSL);
    }
    asm volatile("" ::: "memory");
    const u32x4 qa4 = *(const u32x4*)&sm.qv[4 * c], qb4 = *(const u32x4*)&sm.qv[64 + 4 * c];
    const u64 n1 = uni64(sm.n1);
    const u32 nhi = (u32)(n1 >> 32), nlo = (u32)n1;
    float acc[8], m = -INFINITY, l = 0.f, sc[4];
#pragma unroll
    for (int k = 0; k < 8; ++k) acc[k] = 0.f;
#define FLUSH(AB) do { const float t0_ = sw32_sum2(acc[0], acc[4]), t1_ = sw32_sum2(acc[1], acc[5]), t2_ = sw32_sum2(acc[2], acc[6]), t3_ = sw32_sum2(acc[3], acc[7]); \
        *(f32x2*)&sm.part[AB][wave][8 * c + 2 * r] = f32x2{sw16_sum2(t0_, t2_), sw16_sum2(t1_, t3_)}; } while (0)
#define PUTML(AB) do { l = xrow_sum(l); if (lane == 0) *(f32x2*)&sm.ml[AB][wave][0] = f32x2{m, l}; } while (0)
#define TS(n) ((n) < 8 ? (n) + 16 : (n) < 24 ? (n) - 8 : (n))
#define NEXT(n) do { __builtin_amdgcn_sched_barrier(0); \
        if (TS(n) == FA1) __builtin_amdgcn_s_setprio(2); if (TS(n) == FA2) __builtin_amdgcn_s_setprio(1); if (TS(n) == FA3) __builtin_amdgcn_s_setprio(0); \
        if ((n) >= 8 && (n) + PF < 24) buf[(n) + PF] = LD((n) + PF); \
        if (TS(n) + PFV >= 24 && TS(n) + PFV < NL) buf[TS(n) + PFV] = LD(TS(n) + PFV); \
        __builtin_amdgcn_sched_barrier(0); } while (0)
#define SCORE(B, Q) ({ float d_ = dot2((B)[0], (Q)[0], 0.f); d_ = dot2((B)[1], (Q)[1], d_); d_ = dot2((B)[2], (Q)[2], d_); d_ = dot2((B)[3], (Q)[3], d_); \
                       d_ = row16_sum(d_); bf16r(bf16r(d_) * SCALE); })
#define PV(B, P) do { _Pragma("unroll") for (int k = 0; k < 4; ++k) { acc[2 * k] = fmaf((P), lo16((B)[k]), acc[2 * k]); acc[2 * k + 1] = fmaf((P), hi16((B)[k]), acc[2 * k + 1]); } \
        asm volatile("" : "+v"(acc[0]), "+v"(acc[1]), "+v"(acc[2]), "+v"(acc[3]), "+v"(acc[4]), "+v"(acc[5]), "+v"(acc[6]), "+v"(acc[7]) :: "memory"); } while (0)
    const bool xch = kK != kV;
    u32 pk[8];
#pragma unroll
    for (int n = 8; n < 24; ++n) {
        const u32 sb = __float_as_uint(SCORE(buf[n], qa4));
        if (n & 1) pk[(n - 8) >> 1] |= sb & 0xffff0000u; else pk[(n - 8) >> 1] = sb >> 16;
        asm volatile("" : "+v"(pk[(n - 8) >> 1]) :: "memory");
        if (n == 23 && xch) {
            u32 w0 = pk[0], w1 = pk[1];
#pragma unroll
            for (int k = 1; k < 4; ++k) { w0 = c == k ? pk[2 * k] : w0; w1 = c == k ? pk[2 * k + 1] : w1; }
            if (c < 4) st_sc1(rws, S_OFF + ((h * 8 + kK) * 16 + wave) * 256 + c * 64 + r * 16, u32x4{w0, w1, nhi, nlo});
        }
        NEXT(n);
    }
#pragma unroll
    for (int n = 0; n < 4; ++n) { sc[n] = SCORE(buf[n], qb4) * LOG2E; asm volatile("" : "+v"(sc[n]) :: "memory"); NEXT(n); }
    m = xrow_max(fmaxf(fmaxf(sc[0], sc[1]), fmaxf(sc[2], sc[3])));
#pragma unroll
    for (int n = 4; n < 8; ++n) { const float p = __builtin_amdgcn_exp2f(sc[n & 3] - m); l += p; PV(buf[n], p); NEXT(n); }
    {
        PUTML(1);
        FLUSH(1);
        u32 old = 0;
        lds_wait();
        if (lane == 0) old = __hip_atomic_fetch_add(&sm.cnt[1], 1u, __ATOMIC_RELAXED, __HIP_MEMORY_SCOPE_WORKGROUP);
        if (__builtin_amdgcn_readfirstlane(old) == NW - 1) { asm volatile("" ::: "memory"); combine_publish(sm, 1, lane, rws, h, j, nhi, nlo); }
        l = 0.f;
#pragma unroll
        for (int k = 0; k < 8; ++k) acc[k] = 0.f;
        __builtin_amdgcn_sched_barrier(0);
    }
    const u32 sb = __builtin_amdgcn_readfirstlane(S_OFF + ((h * 8 + kV) * 16 + wave) * 256);
    m = -INFINITY;
    u32 pend = 0;
    if (xch) {
        u32x16 d[4];
        asm volatile("s_load_dwordx16 %0, %4, 0x0 glc\n\ts_load_dwordx16 %1, %4, 0x40 glc\n\ts_load_dwordx16 %2, %4, 0x80 glc\n\ts_load_dwordx16 %3, %4, 0xc0 glc\n\ts_waitcnt lgkmcnt(0)"
                     : "=&s"(d[0]), "=&s"(d[1]), "=&s"(d[2]), "=&s"(d[3]) : "s"(ws + sb) : "memory");
#pragma unroll
        for (int i = 0; i < 4; ++i) {
            pend |= (tbad(d[i], nhi, nlo) != 0) << i;
#pragma unroll
            for (int k = 0; k < 4; ++k) { pk[2 * i] = wrlane(d[i][4 * k], 16 * k, pk[2 * i]); pk[2 * i + 1] = wrlane(d[i][4 * k + 1], 16 * k, pk[2 * i + 1]); }
        }
        asm volatile("s_nop 1" : "+v"(pk[6]), "+v"(pk[7]));
#pragma unroll
        for (int k = 0; k < 8; ++k) pk[k] = __builtin_amdgcn_mov_dpp(pk[k], 0x150, 0xf, 0xf, true);
    }
#pragma unroll
    for (int i = 0; i < 4; ++i) {
        u32 s0 = pk[2 * i], s1 = pk[2 * i + 1];
        if ((pend >> i) & 1) {
            bool ok = false;
            u32x16 d;
#pragma nounroll
            for (u32 spin = 0; spin < SPOLL; ++spin) {
                d = sld16(ws + sb + i * 64);
                if (tbad(d, nhi, nlo) == 0) { ok = true; break; }
                __builtin_amdgcn_s_sleep(SSL);
            }
            if (ok) {
#pragma unroll
                for (int k = 0; k < 4; ++k) { s0 = wrlane(d[4 * k], 16 * k, s0); s1 = wrlane(d[4 * k + 1], 16 * k, s1); }
            } else {
                u32 soff = sb + i * 64 + r * 16;
                u32x4 g;
#pragma nounroll
                for (u32 spin = 0; spin < SPIN_MAX; ++spin) {
                    asm volatile("" : "+v"(soff));
                    g = ld_sc1(rws, soff);
                    if (__all(bad64(g, nhi, nlo) == 0)) break;
                    __builtin_amdgcn_s_sleep(1);
                }
                s0 = g[0]; s1 = g[1];
            }
            asm volatile("s_nop 1" : "+v"(s0), "+v"(s1));
            s0 = __builtin_amdgcn_mov_dpp(s0, 0x150, 0xf, 0xf, true); s1 = __builtin_amdgcn_mov_dpp(s1, 0x150, 0xf, 0xf, true);
        }
        const float mn = fmaxf(m, xrow_max(fmaxf(fmaxf(lo16(s0), hi16(s0)), fmaxf(lo16(s1), hi16(s1)))) * LOG2E);
        if (i) {
            const float f = __builtin_amdgcn_exp2f(m - mn);
            l *= f;
#pragma unroll
            for (int k = 0; k < 8; ++k) acc[k] *= f;
        }
        m = mn;
        sc[0] = __builtin_amdgcn_exp2f(lo16(s0) * LOG2E - m); sc[1] = __builtin_amdgcn_exp2f(hi16(s0) * LOG2E - m);
        sc[2] = __builtin_amdgcn_exp2f(lo16(s1) * LOG2E - m); sc[3] = __builtin_amdgcn_exp2f(hi16(s1) * LOG2E - m);
        l += (sc[0] + sc[1]) + (sc[2] + sc[3]);
        if (i == 3) PUTML(0);
        __builtin_amdgcn_sched_barrier(0);
#pragma unroll
        for (int n = 24 + 4 * i; n < 28 + 4 * i; ++n) { PV(buf[n], sc[n & 3]); NEXT(n); }
    }
    FLUSH(0);
    {
        u32 old = 0;
        lds_wait();
        if (lane == 0) old = __hip_atomic_fetch_add(&sm.cnt[0], 1u, __ATOMIC_RELAXED, __HIP_MEMORY_SCOPE_WORKGROUP);
        if (__builtin_amdgcn_readfirstlane(old) == NW - 1) {
            asm volatile("" ::: "memory"); __builtin_amdgcn_s_setprio(3); combine_publish(sm, 0, lane, rws, h, kV, nhi, nlo);
        }
    }
    if (wave == 5) {
#pragma nounroll
        for (u32 spin = 0; spin < SPIN_MAX * 16; ++spin) {
            if (lds_ld(&sm.cnt[mat]) >= NW) break;
            __builtin_amdgcn_s_sleep(GSL2);
        }
        __builtin_amdgcn_s_setprio(3);
        u32 off = R_OFF + ((mat * 32 + h) * 2 + rh) * 4096 + lo;
        u32x4 g[4];
#pragma nounroll
        for (u32 spin = 0; spin < SPIN_MAX; ++spin) {
            asm volatile("" : "+v"(off));
#pragma unroll
            for (int i = 0; i < 4; ++i) if (spin == 0 || bad48(g[i], nhi, nlo)) g[i] = ld_sc1(rws, off + i * 1024);
            if (__all((bad48(g[0], nhi, nlo) | bad48(g[1], nhi, nlo) | bad48(g[2], nhi, nlo) | bad48(g[3], nhi, nlo)) == 0)) break;
            __builtin_amdgcn_s_sleep(GSLEEP);
        }
        const float W = sw32_max(fmaxf(fmaxf(__uint_as_float(g[0][2]), __uint_as_float(g[1][2])), fmaxf(__uint_as_float(g[2][2]), __uint_as_float(g[3][2]))));
        float o0 = 0.f, o1 = 0.f, S = 0.f;
#pragma unroll
        for (int i = 0; i < 4; ++i) {
            const float e = __builtin_amdgcn_exp2f(__uint_as_float(g[i][2]) - W);
            S += e; o0 = fmaf(e, __uint_as_float(g[i][0] & ~255u), o0); o1 = fmaf(e, __uint_as_float(g[i][1] & ~255u), o1);
        }
        const float o = sw32_sum2(o0, o1) * __builtin_amdgcn_rcpf(sw32_sum2(S, S));
        ((uint16_t*)sm.ov)[2 * (lane & 31) + (lane >> 5)] = (uint16_t)(__float_as_uint(bf16r(o)) >> 16);
        lds_barrier();
        if (b < NRED) {
            const u64 nr = uni64(sm.ep[40]);
            const u32 rhi = (u32)(nr >> 32), rlo = (u32)nr;
            off = Y_OFF + b * 4096 + lo;
#pragma nounroll
            for (u32 spin = 0; spin < SPIN_MAX; ++spin) {
                asm volatile("" : "+v"(off));
#pragma unroll
                for (int i = 0; i < 4; ++i) if (spin == 0 || bad64(g[i], rhi, rlo)) g[i] = ld_sc1(rws, off + i * 1024);
                if (__all((bad64(g[0], rhi, rlo) | bad64(g[1], rhi, rlo) | bad64(g[2], rhi, rlo) | bad64(g[3], rhi, rlo)) == 0)) break;
                __builtin_amdgcn_s_sleep(RSLEEP);
            }
            const float t0 = (__uint_as_float(g[0][0]) + __uint_as_float(g[1][0])) + (__uint_as_float(g[2][0]) + __uint_as_float(g[3][0]));
            const float t1 = (__uint_as_float(g[0][1]) + __uint_as_float(g[1][1])) + (__uint_as_float(g[2][1]) + __uint_as_float(g[3][1]));
            float t = sw32_sum2(t0, t1);
            t = sw16_sum2(t, t); t += dpp<ROR8>(t); t += dpp<ROR4>(t);
            if ((lane & 28) == 0) out[b * 8 + 2 * (lane & 3) + (lane >> 5)] = (uint16_t)(__float_as_uint(bf16r(t)) >> 16);
            if (lane == 0) __hip_atomic_store((u64*)(ws + E_OFF) + b, nr, __ATOMIC_RELAXED, __HIP_MEMORY_SCOPE_AGENT);
        }
    } else if (wave < 11) {
        const int tp = wave < 5 ? wave : wave - 1;
#pragma nounroll
        for (u32 spin = 0; spin < SPIN_MAX * 16; ++spin) {
            if (lds_ld(&sm.cnt[1]) >= NW) break;
            __builtin_amdgcn_s_sleep(GSL2);
        }
        asm volatile("" ::: "memory");
        u32x4 a[2][2];
        const uint16_t* wl = (const uint16_t*)&sm.wo[((tp >> 1) * 8 + r) * 64 + (tp & 1) * 4 + (c >> 3)] + c8;
#pragma unroll
        for (int t = 0; t < 2; ++t)
#pragma unroll
            for (int ks = 0; ks < 2; ++ks)
#pragma unroll
                for (int e = 0; e < 4; ++e) a[t][ks][e] = (u32)wl[(ks * 256 + 2 * e * 8 + t * 2) * 8] | ((u32)wl[(ks * 256 + (2 * e + 1) * 8 + t * 2) * 8] << 16);
        const int red = tp * 4 + (c & 2) + (r >> 1);
        const u64 ny = sm.ep[red < 40 ? red : 0];
        u32 yoff = Y_OFF + ((mat * 80 + ch * 40 + red) * 64 + h * 2 + rh) * 64 + ((r & 1) * 2 + (c & 1)) * 16;
        u32 yhi = (u32)(ny >> 32), ylo = (u32)ny;
        asm volatile("" : "+v"(a[0][0]), "+v"(a[0][1]), "+v"(a[1][0]), "+v"(a[1][1]), "+v"(yoff), "+v"(yhi), "+v"(ylo));
        __builtin_amdgcn_s_setprio(3);
        lds_barrier();
        const u32x4 b0 = sm.ov[r], b1 = sm.ov[4 + r];
        f32x4 d0 = {0.f, 0.f, 0.f, 0.f}, d1 = {0.f, 0.f, 0.f, 0.f};
        d0 = __builtin_amdgcn_mfma_f32_16x16x32_bf16(__builtin_bit_cast(bf16x8, a[0][0]), __builtin_bit_cast(bf16x8, b0), d0, 0, 0, 0);
        d1 = __builtin_amdgcn_mfma_f32_16x16x32_bf16(__builtin_bit_cast(bf16x8, a[1][0]), __builtin_bit_cast(bf16x8, b0), d1, 0, 0, 0);
        d0 = __builtin_amdgcn_mfma_f32_16x16x32_bf16(__builtin_bit_cast(bf16x8, a[0][1]), __builtin_bit_cast(bf16x8, b1), d0, 0, 0, 0);
        d1 = __builtin_amdgcn_mfma_f32_16x16x32_bf16(__builtin_bit_cast(bf16x8, a[1][1]), __builtin_bit_cast(bf16x8, b1), d1, 0, 0, 0);
        const f32x4 d = (c & 2) ? d1 : d0;
        const float y0 = (c & 1) ? d[2] : d[0], y1 = (c & 1) ? d[3] : d[1];
        if (c < 4) st_sc1(rws, yoff, u32x4{__float_as_uint(y0), __float_as_uint(y1), yhi, ylo});
    } else {
        lds_barrier();
        if (wave == 11 && j == 0 && lane == 0) __hip_atomic_store((u64*)(ws + G_OFF + h * 256), n1, __ATOMIC_RELAXED, __HIP_MEMORY_SCOPE_AGENT);
    }
}

extern "C" void kernel_launch(void* const* d_in, const int* in_sizes, int n_in,
                              void* d_out, int out_size, void* d_ws, size_t ws_size,
                              hipStream_t stream) {
    fused<<<NWG, 1024, 0, stream>>>((const uint16_t*)d_in[0], (const uint16_t*)d_in[1], (const uint16_t*)d_in[2], (const uint16_t*)d_in[3], (const uint16_t*)d_in[4],
                                    (const uint16_t*)d_in[5], (const uint16_t*)d_in[6], (const uint16_t*)d_in[7], (const uint16_t*)d_in[8], (const uint16_t*)d_in[9],
                                    (unsigned char*)d_ws, (uint16_t*)d_out);
}
```

```cpp
#include <hip/hip_runtime.h>
#include <hip/hip_bf16.h>
#include <stdint.h>

typedef unsigned int u32;
typedef unsigned long long u64;
typedef __attribute__((ext_vector_type(4))) unsigned int u32x4;
typedef __attribute__((ext_vector_type(2))) unsigned int u32x2;
typedef __attribute__((ext_vector_type(16))) unsigned int u32x16;
typedef __attribute__((ext_vector_type(4))) float f32x4;
typedef __attribute__((ext_vector_type(2))) float f32x2;
typedef __attribute__((ext_vector_type(2))) __bf16 bf16x2;
typedef __attribute__((ext_vector_type(8))) __bf16 bf16x8;
typedef __amdgpu_buffer_rsrc_t rsrc_t;

constexpr int NH = 32, SK = 8192, DH = 128, DM = 640, HD = NH * DH;
constexpr float SCALE = 0.08838834764831845f;
constexpr float LOG2E = 1.4426950408889634f;
constexpr int NWG = 256, NW = 16;
constexpr int NL = 40;
constexpr int NRED = 160;
#define PF 8
#define PFV 6
#define FA1 0
#define FA2 12
#define FA3 24
#define PW 2
#define NPROBE 2
#define GSL2 8
#define DSL 4
#define PCAP 200
#define GSLEEP 1
#define RSLEEP 1
#define YM 0
#define TL 1
#define XS 0
#define XD 0
#define QSL 4
#define SSL 4
constexpr u32 SPOLL = 64;
constexpr u32 SPIN_MAX = 1u << 20;
constexpr u32 E_OFF = 0;
constexpr u32 G_OFF = 2048;
constexpr u32 Q_OFF = 16384;
constexpr u32 R_OFF = Q_OFF + 32 * 1024;
constexpr u32 Y_OFF = R_OFF + 2 * 32 * 2 * 4096;
constexpr u32 D_OFF = Y_OFF + NRED * 64 * 128;
constexpr u32 S_OFF = D_OFF + 32 * 128;
constexpr u32 WS_BYTES = S_OFF + 32 * 8 * 16 * 256;

__device__ __forceinline__ float dot2(u32 a, u32 b, float c) {
    return __builtin_amdgcn_fdot2_f32_bf16(__builtin_bit_cast(bf16x2, a), __builtin_bit_cast(bf16x2, b), c, false);
}
template <int CTRL> __device__ __forceinline__ float dpp(float x) {
    return __builtin_bit_cast(float, __builtin_amdgcn_mov_dpp(__builtin_bit_cast(int, x), CTRL, 0xf, 0xf, true));
}
constexpr int XOR1 = 0xB1, XOR2 = 0x4E, HMIR = 0x141, MIR = 0x140, ROR4 = 0x124, ROR8 = 0x128;
__device__ __forceinline__ float row16_sum(float x) {
    x += dpp<XOR1>(x); x += dpp<XOR2>(x); x += dpp<HMIR>(x); x += dpp<MIR>(x); return x;
}
__device__ __forceinline__ float row16_max(float x) {
    x = fmaxf(x, dpp<XOR1>(x)); x = fmaxf(x, dpp<XOR2>(x)); x = fmaxf(x, dpp<HMIR>(x)); x = fmaxf(x, dpp<MIR>(x)); return x;
}
__device__ __forceinline__ float sw16_sum2(float a, float b) {
    auto s = __builtin_amdgcn_permlane16_swap(__float_as_uint(a), __float_as_uint(b), false, false);
    return __uint_as_float(s[0]) + __uint_as_float(s[1]);
}
__device__ __forceinline__ float sw32_sum2(float a, float b) {
    auto t = __builtin_amdgcn_permlane32_swap(__float_as_uint(a), __float_as_uint(b), false, false);
    return __uint_as_float(t[0]) + __uint_as_float(t[1]);
}
__device__ __forceinline__ float xrow_sum(float x) { x = sw16_sum2(x, x); return sw32_sum2(x, x); }
__device__ __forceinline__ float sw32_max(float x) {
    auto t = __builtin_amdgcn_permlane32_swap(__float_as_uint(x), __float_as_uint(x), false, false);
    return fmaxf(__uint_as_float(t[0]), __uint_as_float(t[1]));
}
__device__ __forceinline__ float xrow_max(float x) {
    auto s = __builtin_amdgcn_permlane16_swap(__float_as_uint(x), __float_as_uint(x), false, false);
    return sw32_max(fmaxf(__uint_as_float(s[0]), __uint_as_float(s[1])));
}
__device__ __forceinline__ float rdlane(float x, int l) { return __builtin_bit_cast(float, __builtin_amdgcn_readlane(__builtin_bit_cast(int, x), l)); }
__device__ __forceinline__ float bf16r(float x) { return (float)(__bf16)x; }
__device__ __forceinline__ float lo16(u32 w) { return __uint_as_float(w << 16); }
__device__ __forceinline__ float hi16(u32 w) { return __uint_as_float(w & 0xffff0000u); }
__device__ __forceinline__ rsrc_t mkrsrc(const void* p, u32 bytes) { return __builtin_amdgcn_make_buffer_rsrc((void*)p, 0, bytes, 0x00020000); }
template <int AUX> __device__ __forceinline__ u32x4 bld(rsrc_t r, u32 off, u32 soff = 0) { return __builtin_amdgcn_raw_buffer_load_b128(r, off, soff, AUX); }
__device__ __forceinline__ u32x4 ld_sc1(rsrc_t r, u32 off, u32 soff = 0) { return __builtin_amdgcn_raw_buffer_load_b128(r, off, soff, 16); }
__device__ __forceinline__ void st_sc1(rsrc_t r, u32 off, u32x4 v) { __builtin_amdgcn_raw_buffer_store_b128(v, r, off, 0, 16); }
__device__ __forceinline__ void lds_barrier() { asm volatile("s_waitcnt lgkmcnt(0)\n\ts_barrier" ::: "memory"); }
__device__ __forceinline__ void lds_wait() { asm volatile("s_waitcnt lgkmcnt(0)" ::: "memory"); }
template <int N> __device__ __forceinline__ void vm_wait() { __builtin_amdgcn_s_waitcnt(0x0f70 | (N & 15) | ((N >> 4) << 14)); }
__device__ __forceinline__ u64 uni64(u64 v) { return ((u64)(u32)__builtin_amdgcn_readfirstlane((u32)(v >> 32)) << 32) | (u32)__builtin_amdgcn_readfirstlane((u32)v); }

__device__ __forceinline__ u64 sld64(const void* p) { u64 v; asm volatile("s_load_dwordx2 %0, %1, 0x0 glc\n\ts_waitcnt lgkmcnt(0)" : "=&s"(v) : "s"(p) : "memory"); return v; }
__device__ __forceinline__ u32x16 sld16(const void* p) { u32x16 v; asm volatile("s_load_dwordx16 %0, %1, 0x0 glc\n\ts_waitcnt lgkmcnt(0)" : "=&s"(v) : "s"(p) : "memory"); return v; }
__device__ __forceinline__ u32 tbad(const u32x16& d, u32 hi, u32 lo) {
    return ((d[2] ^ hi) | (d[3] ^ lo)) | ((d[6] ^ hi) | (d[7] ^ lo)) | ((d[10] ^ hi) | (d[11] ^ lo)) | ((d[14] ^ hi) | (d[15] ^ lo));
}
__device__ __forceinline__ u32 wrlane(u32 v, int l, u32 old) { asm("v_writelane_b32 %0, %1, %2" : "+v"(old) : "s"(v), "n"(l)); return old; }
__device__ __forceinline__ void glds16(const void* gsrc, u32 lds_dst) {
    u32 keep;
    asm volatile("s_mov_b32 %0, m0\n\ts_mov_b32 m0, %2\n\ts_nop 0\n\tglobal_load_lds_dwordx4 %1, off nt\n\ts_mov_b32 m0, %0" : "=&s"(keep) : "v"(gsrc), "s"(lds_dst) : "memory");
}
__device__ __forceinline__ u32 lds_ld(const u32* p) { return __builtin_amdgcn_readfirstlane(__hip_atomic_load(p, __ATOMIC_RELAXED, __HIP_MEMORY_SCOPE_WORKGROUP)); }
__device__ __forceinline__ u32 bad48(u32x4 g, u32 hi, u32 lo) { return (((g[0] ^ hi) | (g[1] ^ (hi >> 8))) & 255u) | (g[3] ^ lo); }
__device__ __forceinline__ u32 bad64(u32x4 g, u32 hi, u32 lo) { return (g[2] ^ hi) | (g[3] ^ lo); }

struct Smem {
    u32x4 wo[40 * 64];
    float part[2][NW][128];
    float ml[2][NW][2];
    u32x4 ov[8];
    float qp[4][32];
    u64 ep[48], n1;
    u32 qv[128];
    u32 qcnt, q4, cnt[2], dec, pdone;
};

__device__ __forceinline__ void combine_publish(Smem& sm, int ab, int lane, rsrc_t rws, int h, int slot, u32 nhi, u32 nlo) {
    const f32x2 ml = *(const f32x2*)&sm.ml[ab][lane & 15][0];
    f32x2 pv[NW];
#pragma unroll
    for (int w = 0; w < NW; ++w) pv[w] = *(const f32x2*)&sm.part[ab][w][2 * lane];
    const u32 off = R_OFF + (((ab * 32 + h) * 2 + (lane >> 5)) * 8 + slot) * 512 + (lane & 31) * 16;
    __builtin_amdgcn_sched_barrier(0);
    const float M = row16_max(ml[0]);
    const float f = __builtin_amdgcn_exp2f(ml[0] - M);
    const float L = row16_sum(f * ml[1]);
    f32x2 o[4];
#pragma unroll
    for (int w = 0; w < NW; ++w) { if (w < 4) o[w] = rdlane(f, w) * pv[w]; else o[w & 3] += rdlane(f, w) * pv[w]; }
    const f32x2 t = ((o[0] + o[1]) + (o[2] + o[3])) * __builtin_amdgcn_rcpf(L);
    const float W = M + __builtin_amdgcn_logf(L);
    st_sc1(rws, off, u32x4{(__float_as_uint(t[0]) & ~255u) | (nhi & 255u), (__float_as_uint(t[1]) & ~255u) | ((nhi >> 8) & 255u), __float_as_uint(W), nlo});
}

__global__ void __launch_bounds__(1024) fused(const uint16_t* __restrict__ xA, const uint16_t* __restrict__ kA, const uint16_t* __restrict__ vA,
                                              const uint16_t* __restrict__ wqA, const uint16_t* __restrict__ woA,
                                              const uint16_t* __restrict__ xB, const uint16_t* __restrict__ kB, const uint16_t* __restrict__ vB,
                                              const uint16_t* __restrict__ wqB, const uint16_t* __restrict__ woB,
                                              unsigned char* __restrict__ ws, uint16_t* __restrict__ out) {
    __shared__ __attribute__((aligned(16))) Smem sm;
    const int b = blockIdx.x;
    const int h = b >> 3, j = b & 7;
    const int tid = threadIdx.x, lane = tid & 63, wave = __builtin_amdgcn_readfirstlane(tid >> 6), r = lane >> 4, c = lane & 15;
    const int row8 = lane >> 3, c8 = lane & 7;
    const int mat = j >> 2, rh = (j >> 1) & 1, ch = j & 1;
    const rsrc_t rws = mkrsrc(ws, WS_BYTES);
    const rsrc_t rkA = mkrsrc(kA, NH * SK * DH * 2), rvA = mkrsrc(vA, NH * SK * DH * 2);
    const rsrc_t rkB = mkrsrc(kB, NH * SK * DH * 2), rvB = mkrsrc(vB, NH * SK * DH * 2);
    const u64 tsp0 = __builtin_amdgcn_s_memrealtime();
    const u32 lo = lane * 16;
#define CLS(k) ((u32)((((k) & 1) << 14) | (((k) & 2) << 11) | (((k) & 4) << 11)))
    const u32 soB = (u32)h * (SK * 256) + ((48 + wave) << 15) + CLS(j ^ (wave & 7));
    u32x4 buf[NL];
#define GX(i) ((u32)(((i) << 19) | (((i) & 1) << 14) | (((i) >> 1) << 12)))
#define LD(n) ((n) < 8 ? bld<2>(((n) & 4) ? rvB : rkB, lo + ((n) & 3) * 1024, soB) : (n) < 24 ? bld<2>(rkA, lo + ((n) & 3) * 1024, soK ^ GX(((n) - 8) >> 2)) : bld<2>(rvA, lo + ((n) & 3) * 1024, soV ^ GX(((n) - 24) >> 2)))
    __builtin_amdgcn_s_setprio(3);
    u32 soK = 0, soV = 0;
    if (wave < 4) {
        const int hq = (h & ~1) | (j >> 2), qm = (j >> 1) & 1, qd0 = (j & 1) * 64 + (h & 1) * 32;
        const rsrc_t rw = mkrsrc(qm ? wqB : wqA, DM * HD * 2), rx = mkrsrc(qm ? xB : xA, DM * 2);
        const u32 wb = (wave * 160 * HD + hq * DH + qd0) * 2;
        const u32x4 x4 = bld<0>(rx, 320 * wave + 20 * (lane >> 2));
        const u32 x1 = __builtin_amdgcn_raw_buffer_load_b32(rx, 320 * wave + 20 * (lane >> 2) + 16, 0, 0);
        u32x4 w[10];
#pragma unroll
        for (int i = 0; i < 10; ++i) w[i] = bld<2>(rw, ((lane >> 2) * 10 * HD + (lane & 3) * 8) * 2, wb + i * HD * 2);
        __builtin_amdgcn_sched_barrier(0);
        lds_barrier();
        const u64 nq = sld64(ws + G_OFF + hq * 256) + 1;
        __builtin_amdgcn_sched_barrier(0);
        float q[8];
#pragma unroll
        for (int k = 0; k < 8; ++k) q[k] = 0.f;
#pragma unroll
        for (int i = 0; i < 10; ++i) {
            const u32 xw = i < 8 ? x4[i >> 1] : x1;
            const float xs = (i & 1) ? hi16(xw) : lo16(xw);
#pragma unroll
            for (int k = 0; k < 4; ++k) { q[2 * k] = fmaf(xs, lo16(w[i][k]), q[2 * k]); q[2 * k + 1] = fmaf(xs, hi16(w[i][k]), q[2 * k + 1]); }
        }
#pragma unroll
        for (int k = 0; k < 8; ++k) { q[k] += dpp<ROR4>(q[k]); q[k] += dpp<ROR8>(q[k]); q[k] = xrow_sum(q[k]); }
        if (lane < 4) { *(f32x4*)&sm.qp[wave][8 * lane] = f32x4{q[0], q[1], q[2], q[3]}; *(f32x4*)&sm.qp[wave][8 * lane + 4] = f32x4{q[4], q[5], q[6], q[7]}; }
        u32 old = 0;
        lds_wait();
        if (lane == 0) old = __hip_atomic_fetch_add(&sm.q4, 1u, __ATOMIC_RELAXED, __HIP_MEMORY_SCOPE_WORKGROUP);
        if (__builtin_amdgcn_readfirstlane(old) == 3) {
            asm volatile("" ::: "memory");
            const f32x4 s = (*(const f32x4*)&sm.qp[0][4 * c8] + *(const f32x4*)&sm.qp[1][4 * c8]) + (*(const f32x4*)&sm.qp[2][4 * c8] + *(const f32x4*)&sm.qp[3][4 * c8]);
            const u32 p0 = (__float_as_uint(bf16r(s[0])) >> 16) | (__float_as_uint(bf16r(s[1])) & 0xffff0000u);
            const u32 p1 = (__float_as_uint(bf16r(s[2])) >> 16) | (__float_as_uint(bf16r(s[3])) & 0xffff0000u);
            if (lane < 8) st_sc1(rws, Q_OFF + hq * 1024 + (qm * 128 + qd0) * 4 + lo, u32x4{p0, p1, (u32)(nq >> 32), (u32)nq});
        }
#pragma nounroll
        for (u32 spin = 0; spin < SPIN_MAX * 16; ++spin) {
            if (lds_ld(&sm.pdone)) break;
            __builtin_amdgcn_s_sleep(2);
        }
        asm volatile("" ::: "memory");
        {
            const u64 n1 = uni64(sm.n1);
            const u32 nhi = (u32)(n1 >> 32), nlo = (u32)n1;
            const u32 qb = __builtin_amdgcn_readfirstlane(Q_OFF + h * 1024 + wave * 256);
            bool ok = false;
            u32x16 d0, d1, d2, d3;
#pragma nounroll
            for (u32 spin = 0; spin < SPOLL; ++spin) {
                asm volatile("s_load_dwordx16 %0, %4, 0x0 glc\n\ts_load_dwordx16 %1, %4, 0x40 glc\n\ts_load_dwordx16 %2, %4, 0x80 glc\n\ts_load_dwordx16 %3, %4, 0xc0 glc\n\ts_waitcnt lgkmcnt(0)"
                             : "=&s"(d0), "=&s"(d1), "=&s"(d2), "=&s"(d3) : "s"(ws + qb) : "memory");
                if (((tbad(d0, nhi, nlo) | tbad(d1, nhi, nlo)) | (tbad(d2, nhi, nlo) | tbad(d3, nhi, nlo))) == 0) { ok = true; break; }
                __builtin_amdgcn_s_sleep(QSL);
            }
            u32 g0 = 0, g1 = 0;
            if (ok) {
#pragma unroll
                for (int i = 0; i < 4; ++i) {
                    g0 = wrlane(d0[4 * i], i, g0); g1 = wrlane(d0[4 * i + 1], i, g1);
                    g0 = wrlane(d1[4 * i], 4 + i, g0); g1 = wrlane(d1[4 * i + 1], 4 + i, g1);
                    g0 = wrlane(d2[4 * i], 8 + i, g0); g1 = wrlane(d2[4 * i + 1], 8 + i, g1);
                    g0 = wrlane(d3[4 * i], 12 + i, g0); g1 = wrlane(d3[4 * i + 1], 12 + i, g1);
                }
            } else {
                u32 off = qb + c * 16;
                u32x4 g;
#pragma nounroll
                for (u32 spin = 0; spin < SPIN_MAX; ++spin) {
                    asm volatile("" : "+v"(off));
                    g = ld_sc1(rws, off);
                    if (__all(bad64(g, nhi, nlo) == 0)) break;
                    __builtin_amdgcn_s_sleep(1);
                }
                g0 = g[0]; g1 = g[1];
            }
            if (lane < 16) *(u32x2*)&sm.qv[wave * 32 + 2 * lane] = u32x2{g0, g1};
            lds_wait();
            if (lane == 0) __hip_atomic_fetch_add(&sm.qcnt, 1u, __ATOMIC_RELAXED, __HIP_MEMORY_SCOPE_WORKGROUP);
        }
        __builtin_amdgcn_sched_barrier(0);
#pragma unroll
        for (int n = 0; n < 8; ++n) {
            if (n >= PW && lds_ld(&sm.dec) == 0) vm_wait<PW - 1>();
            buf[n] = LD(n);
            __builtin_amdgcn_sched_barrier(0);
        }
    } else if (wave == 12) {
        if (lane == 0) { sm.qcnt = 0; sm.q4 = 0; sm.cnt[0] = 0; sm.cnt[1] = 0; sm.dec = 0; sm.pdone = 0; }
        lds_barrier();
        int gs = 0;
        {
            const unsigned char* pa = (const unsigned char*)((j & 1) ? vA : kA) + (size_t)h * (SK * 256) + (j >> 1) * (16 * 32768);
#pragma unroll
            for (int i = 0; i < NPROBE; ++i) {
                const int side = ((j >> 1) ^ i) & 1;
                const unsigned char* p1 = pa + i * 32768 + side * 8192;
                const unsigned char* p2 = pa + i * 32768 + (side ^ 1) * 8192;
                u32 v1, v2; u64 t1, t2;
                asm volatile("s_load_dword %0, %4, 0x0 glc\n\ts_load_dword %1, %5, 0x0 glc\n\ts_waitcnt lgkmcnt(1)\n\ts_memtime %2\n\ts_waitcnt lgkmcnt(0)\n\ts_memtime %3\n\ts_waitcnt lgkmcnt(0)"
                             : "=&s"(v1), "=&s"(v2), "=&s"(t1), "=&s"(t2) : "s"(p1), "s"(p2) : "memory");
                const int g = min((int)(u32)(t2 - t1), PCAP);
                gs += side ? -g : g;
            }
        }
        u64 e1;
        {
            u32 dummy;
            asm volatile("s_load_dword %1, %3, 0x0\n\ts_load_dwordx2 %0, %2, 0x0 glc\n\ts_waitcnt lgkmcnt(0)" : "=&s"(e1), "=&s"(dummy)
                         : "s"(ws + G_OFF + h * 256), "s"((const unsigned char*)((j & 1) ? kA : vA) + (size_t)h * (SK * 256) + j * 4096) : "memory");
        }
        const u64 n1 = e1 + 1;
        const u32 nhi = (u32)(n1 >> 32), nlo = (u32)n1;
        const int iod0 = (__builtin_amdgcn_s_getreg((20 << 0) | (0 << 6) | (3 << 11)) >> 2) & 1;
        if (lane == 0) st_sc1(rws, D_OFF + h * 128 + j * 16, u32x4{(u32)(iod0 ? -gs : gs), (u32)iod0, nhi, nlo});
        if (lane == 0) { sm.n1 = n1; lds_wait(); __hip_atomic_store(&sm.pdone, 1u, __ATOMIC_RELAXED, __HIP_MEMORY_SCOPE_WORKGROUP); }
        int vg[8], vi[8];
        bool ok = false;
#pragma nounroll
        for (u32 spin = 0; spin < SPOLL; ++spin) {
            __builtin_amdgcn_s_sleep(2);
            u32x16 d0, d1;
            asm volatile("s_load_dwordx16 %0, %2, 0x0 glc\n\ts_load_dwordx16 %1, %2, 0x40 glc\n\ts_waitcnt lgkmcnt(0)" : "=&s"(d0), "=&s"(d1) : "s"(ws + (u32)__builtin_amdgcn_readfirstlane(D_OFF + h * 128)) : "memory");
#pragma unroll
            for (int i = 0; i < 4; ++i) { vg[i] = d0[4 * i]; vi[i] = d0[4 * i + 1]; vg[4 + i] = d1[4 * i]; vi[4 + i] = d1[4 * i + 1]; }
            if ((tbad(d0, nhi, nlo) | tbad(d1, nhi, nlo)) == 0) { ok = true; break; }
        }
        if (!ok) {
            u32 off = D_OFF + h * 128 + c8 * 16;
            u32x4 g;
#pragma nounroll
            for (u32 spin = 0; spin < SPIN_MAX; ++spin) {
                asm volatile("" : "+v"(off));
                g = ld_sc1(rws, off);
                if (__all(bad64(g, nhi, nlo) == 0)) break;
                __builtin_amdgcn_s_sleep(1);
            }
#pragma unroll
            for (int i = 0; i < 8; ++i) { vg[i] = __builtin_amdgcn_readlane(g[0], i); vi[i] = __builtin_amdgcn_readlane(g[1], i); }
        }
        int msk = 0;
#pragma unroll
        for (int i = 0; i < 8; ++i) msk |= (vi[i] & 1) << i;
        const int sk = (vg[0] + vg[2]) + (vg[4] + vg[6]), sv = (vg[1] + vg[3]) + (vg[5] + vg[7]);
        const int iod = (msk >> j) & 1;
        const int rank = __builtin_popcount((iod ? msk : ~msk) & ((1 << j) - 1));
        const int pk_ = sk > 0 ? 0 : 1, pv_ = sv > 0 ? 0 : 1;
        const bool bal = __builtin_popcount(msk) == 4;
        const u32 d = 0x100u | (bal ? ((iod ^ pk_) * 4 + rank) | (((iod ^ pv_) * 4 + rank) << 4) : j * 17);
        if (lane == 0) __hip_atomic_store(&sm.dec, d, __ATOMIC_RELAXED, __HIP_MEMORY_SCOPE_WORKGROUP);
        __builtin_amdgcn_sched_barrier(0);
#pragma unroll
        for (int n = 0; n < 8; ++n) { buf[n] = LD(n); __builtin_amdgcn_sched_barrier(0); }
    } else {
        lds_barrier();
        if (wave < 12) {
            const uint16_t* wsrc = (mat ? woB : woA) + (size_t)(h * DH + rh * 64 + row8) * DM + ch * 320 + c8 * 8;
#pragma unroll
            for (int i = 0; i < 5; ++i) {
                const int idx = (wave - 4) * 5 + i;
                if (i >= PW) vm_wait<PW - 1>();
                glds16(wsrc + (idx & 7) * 8 * DM + (idx >> 3) * 64, (u32)(uintptr_t)&sm.wo[idx * 64]);
            }
        }
        __builtin_amdgcn_sched_barrier(0);
        const u64 ev = __hip_atomic_load((const u64*)(ws + E_OFF) + (lane < 40 ? mat * 80 + ch * 40 + lane : b < NRED ? b : 0), __ATOMIC_RELAXED, __HIP_MEMORY_SCOPE_AGENT);
#pragma unroll
        for (int n = 0; n < 8; ++n) {
            if (lds_ld(&sm.dec) == 0) vm_wait<PW - 1>();
            buf[n] = LD(n);
            __builtin_amdgcn_sched_barrier(0);
        }
        if (wave == 8 && lane < 48) sm.ep[lane] = ev + 1;
    }
    __builtin_amdgcn_sched_barrier(0);
    u32 dec = 0;
#pragma nounroll
    for (u32 spin = 0; spin < SPIN_MAX * 16; ++spin) {
        dec = lds_ld(&sm.dec);
        if (dec) break;
        __builtin_amdgcn_s_sleep(DSL);
    }
    const int kK = dec & 7, kV = (dec >> 4) & 7;
    soK = (u32)h * (SK * 256) + (wave << 15) + CLS(kK);
    soV = (u32)h * (SK * 256) + (wave << 15) + CLS(kV);
    __builtin_amdgcn_sched_barrier(0);
#pragma unroll
    for (int n = 8; n < 8 + PF; ++n) { buf[n] = LD(n); __builtin_amdgcn_sched_barrier(0); }
    __builtin_amdgcn_sched_barrier(0);
#pragma nounroll
    for (u32 spin = 0; spin < SPIN_MAX * 16; ++spin) {
        if (lds_ld(&sm.qcnt) == 4) break;
        __builtin_amdgcn_s_sleep(DSL);
    }
    asm volatile("" ::: "memory");
    const u32x4 qa4 = *(const u32x4*)&sm.qv[4 * c], qb4 = *(const u32x4*)&sm.qv[64 + 4 * c];
    const u64 n1 = uni64(sm.n1);
    const u32 nhi = (u32)(n1 >> 32), nlo = (u32)n1;
    float acc[8], m = -INFINITY, l = 0.f, sc[4];
#pragma unroll
    for (int k = 0; k < 8; ++k) acc[k] = 0.f;
#define FLUSH(AB) do { const float t0_ = sw32_sum2(acc[0], acc[4]), t1_ = sw32_sum2(acc[1], acc[5]), t2_ = sw32_sum2(acc[2], acc[6]), t3_ = sw32_sum2(acc[3], acc[7]); \
        *(f32x2*)&sm.part[AB][wave][8 * c + 2 * r] = f32x2{sw16_sum2(t0_, t2_), sw16_sum2(t1_, t3_)}; } while (0)
#define PUTML(AB) do { l = xrow_sum(l); if (lane == 0) *(f32x2*)&sm.ml[AB][wave][0] = f32x2{m, l}; } while (0)
#define TS(n) ((n) < 8 ? (n) + 16 : (n) < 24 ? (n) - 8 : (n))
#define NEXT(n) do { __builtin_amdgcn_sched_barrier(0); \
        if (TS(n) == FA1) __builtin_amdgcn_s_setprio(2); if (TS(n) == FA2) __builtin_amdgcn_s_setprio(1); if (TS(n) == FA3) __builtin_amdgcn_s_setprio(0); \
        if ((n) >= 8 && (n) + PF < 24) buf[(n) + PF] = LD((n) + PF); \
        if (TS(n) + PFV >= 24 && TS(n) + PFV < NL) buf[TS(n) + PFV] = LD(TS(n) + PFV); \
        if (XS && TS(n) == 15 + XS) { __builtin_amdgcn_sched_barrier(0); st_sc1(mkrsrc(ws, xch ? WS_BYTES : 0), c < 4 ? S_OFF + ((h * 8 + kK) * 16 + wave) * 256 + c * 64 + r * 16 : WS_BYTES, u32x4{xw0, xw1, nhi, nlo}); } \
        __builtin_amdgcn_sched_barrier(0); } while (0)
#define SCORE(B, Q) ({ float d_ = dot2((B)[0], (Q)[0], 0.f); d_ = dot2((B)[1], (Q)[1], d_); d_ = dot2((B)[2], (Q)[2], d_); d_ = dot2((B)[3], (Q)[3], d_); \
                       d_ = row16_sum(d_); bf16r(bf16r(d_) * SCALE); })
#define PV(B, P) do { _Pragma("unroll") for (int k = 0; k < 4; ++k) { acc[2 * k] = fmaf((P), lo16((B)[k]), acc[2 * k]); acc[2 * k + 1] = fmaf((P), hi16((B)[k]), acc[2 * k + 1]); } \
        asm volatile("" : "+v"(acc[0]), "+v"(acc[1]), "+v"(acc[2]), "+v"(acc[3]), "+v"(acc[4]), "+v"(acc[5]), "+v"(acc[6]), "+v"(acc[7]) :: "memory"); } while (0)
    const bool xch = kK != kV;
    u32 pk[8], xw0 = 0, xw1 = 0;
#pragma unroll
    for (int n = 8; n < 24; ++n) {
        const u32 sb = __float_as_uint(SCORE(buf[n], qa4));
        if (n & 1) pk[(n - 8) >> 1] |= sb & 0xffff0000u; else pk[(n - 8) >> 1] = sb >> 16;
        asm volatile("" : "+v"(pk[(n - 8) >> 1]) :: "memory");
        if (n == 23 && XS) {
            xw0 = pk[0]; xw1 = pk[1];
#pragma unroll
            for (int k = 1; k < 4; ++k) { xw0 = c == k ? pk[2 * k] : xw0; xw1 = c == k ? pk[2 * k + 1] : xw1; }
        }
        if (n == 23 && xch && !XS && XD != 3 && XD != 4 && XD != 5) {
            u32 w0 = pk[0], w1 = pk[1];
#pragma unroll
            for (int k = 1; k < 4; ++k) { w0 = c == k ? pk[2 * k] : w0; w1 = c == k ? pk[2 * k + 1] : w1; }
            if (XD == 8) { if ((wave & 3) == 3) st_sc1(rws, S_OFF + ((h * 8 + kK) * 16 + (wave & 12)) * 256 + lo, u32x4{w0, w1, nhi, nlo}); }
            else if (XD == 9) { if (wave == 15) { for (int k = 0; k < 4; ++k) st_sc1(rws, S_OFF + ((h * 8 + kK) * 16) * 256 + k * 1024 + lo, u32x4{w0, w1, nhi, nlo}); } }
            else if (XD == 10) { if (wave == 15) { for (int k = 0; k < 2; ++k) st_sc1(rws, S_OFF + ((h * 8 + kK) * 16) * 256 + k * 1024 + lo, u32x4{w0, w1, nhi, nlo}); } }
            else
            if (c < (XD == 6 ? 2 : XD == 7 ? 1 : 4)) st_sc1(rws, S_OFF + ((h * 8 + kK) * 16 + (XD == 2 ? 0 : wave)) * 256 + c * 64 + r * 16, u32x4{w0, w1, nhi, nlo});
        }
        NEXT(n);
    }
#pragma unroll
    for (int n = 0; n < 4; ++n) { sc[n] = SCORE(buf[n], qb4) * LOG2E; asm volatile("" : "+v"(sc[n]) :: "memory"); NEXT(n); }
    m = xrow_max(fmaxf(fmaxf(sc[0], sc[1]), fmaxf(sc[2], sc[3])));
#pragma unroll
    for (int n = 4; n < 8; ++n) { const float p = __builtin_amdgcn_exp2f(sc[n & 3] - m); l += p; PV(buf[n], p); NEXT(n); }
    {
        PUTML(1);
        FLUSH(1);
        u32 old = 0;
        lds_wait();
        if (lane == 0) old = __hip_atomic_fetch_add(&sm.cnt[1], 1u, __ATOMIC_RELAXED, __HIP_MEMORY_SCOPE_WORKGROUP);
        if (__builtin_amdgcn_readfirstlane(old) == NW - 1) { asm volatile("" ::: "memory"); combine_publish(sm, 1, lane, rws, h, j, nhi, nlo); }
        l = 0.f;
#pragma unroll
        for (int k = 0; k < 8; ++k) acc[k] = 0.f;
        __builtin_amdgcn_sched_barrier(0);
    }
    const u32 sb = __builtin_amdgcn_readfirstlane(S_OFF + ((h * 8 + kV) * 16 + (XD == 5 ? 0 : wave)) * 256);
    m = -INFINITY;
    u32 pend = 0;
    if (xch && (XD == 0 || XD == 4 || XD == 5)) {
        u32x16 d[4];
        asm volatile("s_load_dwordx16 %0, %4, 0x0 glc\n\ts_load_dwordx16 %1, %4, 0x40 glc\n\ts_load_dwordx16 %2, %4, 0x80 glc\n\ts_load_dwordx16 %3, %4, 0xc0 glc\n\ts_waitcnt lgkmcnt(0)"
                     : "=&s"(d[0]), "=&s"(d[1]), "=&s"(d[2]), "=&s"(d[3]) : "s"(ws + sb) : "memory");
#pragma unroll
        for (int i = 0; i < 4; ++i) {
            if (XD == 0) pend |= (tbad(d[i], nhi, nlo) != 0) << i;
#pragma unroll
            for (int k = 0; k < 4; ++k) { pk[2 * i] = wrlane(d[i][4 * k], 16 * k, pk[2 * i]); pk[2 * i + 1] = wrlane(d[i][4 * k + 1], 16 * k, pk[2 * i + 1]); }
        }
        asm volatile("s_nop 1" : "+v"(pk[6]), "+v"(pk[7]));
#pragma unroll
        for (int k = 0; k < 8; ++k) pk[k] = __builtin_amdgcn_mov_dpp(pk[k], 0x150, 0xf, 0xf, true);
    }
#pragma unroll
    for (int i = 0; i < 4; ++i) {
        u32 s0 = pk[2 * i], s1 = pk[2 * i + 1];
        if ((pend >> i) & 1) {
            bool ok = false;
            u32x16 d;
#pragma nounroll
            for (u32 spin = 0; spin < SPOLL; ++spin) {
                d = sld16(ws + sb + i * 64);
                if (tbad(d, nhi, nlo) == 0) { ok = true; break; }
                __builtin_amdgcn_s_sleep(SSL);
            }
            if (ok) {
#pragma unroll
                for (int k = 0; k < 4; ++k) { s0 = wrlane(d[4 * k], 16 * k, s0); s1 = wrlane(d[4 * k + 1], 16 * k, s1); }
            } else {
                u32 soff = sb + i * 64 + r * 16;
                u32x4 g;
#pragma nounroll
                for (u32 spin = 0; spin < SPIN_MAX; ++spin) {
                    asm volatile("" : "+v"(soff));
                    g = ld_sc1(rws, soff);
                    if (__all(bad64(g, nhi, nlo) == 0)) break;
                    __builtin_amdgcn_s_sleep(1);
                }
                s0 = g[0]; s1 = g[1];
            }
            asm volatile("s_nop 1" : "+v"(s0), "+v"(s1));
            s0 = __builtin_amdgcn_mov_dpp(s0, 0x150, 0xf, 0xf, true); s1 = __builtin_amdgcn_mov_dpp(s1, 0x150, 0xf, 0xf, true);
        }
        const float mn = fmaxf(m, xrow_max(fmaxf(fmaxf(lo16(s0), hi16(s0)), fmaxf(lo16(s1), hi16(s1)))) * LOG2E);
        if (i) {
            const float f = __builtin_amdgcn_exp2f(m - mn);
            l *= f;
#pragma unroll
            for (int k = 0; k < 8; ++k) acc[k] *= f;
        }
        m = mn;
        sc[0] = __builtin_amdgcn_exp2f(lo16(s0) * LOG2E - m); sc[1] = __builtin_amdgcn_exp2f(hi16(s0) * LOG2E - m);
        sc[2] = __builtin_amdgcn_exp2f(lo16(s1) * LOG2E - m); sc[3] = __builtin_amdgcn_exp2f(hi16(s1) * LOG2E - m);
        l += (sc[0] + sc[1]) + (sc[2] + sc[3]);
        if (i == 3) PUTML(0);
        __builtin_amdgcn_sched_barrier(0);
#pragma unroll
        for (int n = 24 + 4 * i; n < 28 + 4 * i; ++n) { PV(buf[n], sc[n & 3]); NEXT(n); }
    }
    FLUSH(0);
    {
        u32 old = 0;
        lds_wait();
        if (lane == 0) old = __hip_atomic_fetch_add(&sm.cnt[0], 1u, __ATOMIC_RELAXED, __HIP_MEMORY_SCOPE_WORKGROUP);
        if (__builtin_amdgcn_readfirstlane(old) == NW - 1) {
            asm volatile("" ::: "memory"); __builtin_amdgcn_s_setprio(3); combine_publish(sm, 0, lane, rws, h, kV, nhi, nlo);
        }
    }
    if (wave == 5) {
#pragma nounroll
        for (u32 spin = 0; spin < SPIN_MAX * 16; ++spin) {
            if (lds_ld(&sm.cnt[mat]) >= NW) break;
            __builtin_amdgcn_s_sleep(GSL2);
        }
        __builtin_amdgcn_s_setprio(3);
        u32 off = R_OFF + ((mat * 32 + h) * 2 + rh) * 4096 + lo;
        u32x4 g[4];
#pragma unroll
        for (int i = 0; i < 4; ++i) g[i] = ld_sc1(rws, off + i * 1024);
#pragma nounroll
        for (u32 spin = 0; spin < SPIN_MAX; ++spin) {
            bool p[4];
#pragma unroll
            for (int i = 0; i < 4; ++i) p[i] = bad48(g[i], nhi, nlo) != 0;
            if (!__any(p[0] | p[1] | p[2] | p[3])) break;
            if (GSLEEP) __builtin_amdgcn_s_sleep(GSLEEP);
            asm volatile("" : "+v"(off));
#pragma unroll
            for (int i = 0; i < 4; ++i) if (p[i]) g[i] = ld_sc1(rws, off + i * 1024);
        }
        const float W = sw32_max(fmaxf(fmaxf(__uint_as_float(g[0][2]), __uint_as_float(g[1][2])), fmaxf(__uint_as_float(g[2][2]), __uint_as_float(g[3][2]))));
        float o0 = 0.f, o1 = 0.f, S = 0.f;
#pragma unroll
        for (int i = 0; i < 4; ++i) {
            const float e = __builtin_amdgcn_exp2f(__uint_as_float(g[i][2]) - W);
            S += e; o0 = fmaf(e, __uint_as_float(g[i][0] & ~255u), o0); o1 = fmaf(e, __uint_as_float(g[i][1] & ~255u), o1);
        }
        const float o = sw32_sum2(o0, o1) * __builtin_amdgcn_rcpf(sw32_sum2(S, S));
        ((uint16_t*)sm.ov)[2 * (lane & 31) + (lane >> 5)] = (uint16_t)(__float_as_uint(bf16r(o)) >> 16);
        lds_barrier();
        if (b < NRED) {
            const u64 nr = uni64(sm.ep[40]);
            const u32 rhi = (u32)(nr >> 32), rlo = (u32)nr;
            constexpr u32 YS = YM == 1 ? 2048 : 1024;
            off = YM == 1 ? Y_OFF + b * 8192 + (lane >> 2) * 128 + (lane & 3) * 16 : Y_OFF + b * 4096 + lo;
#pragma unroll
            for (int i = 0; i < 4; ++i) g[i] = ld_sc1(rws, off + i * YS);
#pragma nounroll
            for (u32 spin = 0; spin < SPIN_MAX; ++spin) {
                bool p[4];
#pragma unroll
                for (int i = 0; i < 4; ++i) p[i] = bad64(g[i], rhi, rlo) != 0;
                if (!__any(p[0] | p[1] | p[2] | p[3])) break;
                if (RSLEEP) __builtin_amdgcn_s_sleep(RSLEEP);
                asm volatile("" : "+v"(off));
#pragma unroll
                for (int i = 0; i < 4; ++i) if (p[i]) g[i] = ld_sc1(rws, off + i * YS);
            }
            const float t0 = (__uint_as_float(g[0][0]) + __uint_as_float(g[1][0])) + (__uint_as_float(g[2][0]) + __uint_as_float(g[3][0]));
            const float t1 = (__uint_as_float(g[0][1]) + __uint_as_float(g[1][1])) + (__uint_as_float(g[2][1]) + __uint_as_float(g[3][1]));
            float t = sw32_sum2(t0, t1);
            t = sw16_sum2(t, t); t += dpp<ROR8>(t); t += dpp<ROR4>(t);
            if ((lane & 28) == 0) out[b * 8 + 2 * (lane & 3) + (lane >> 5)] = (uint16_t)(__float_as_uint(bf16r(t)) >> 16);
            if (lane == 0) __hip_atomic_store((u64*)(ws + E_OFF) + b, nr, __ATOMIC_RELAXED, __HIP_MEMORY_SCOPE_AGENT);
        }
    } else if (wave < 11) {
        const int tp = wave < 5 ? wave : wave - 1;
#pragma nounroll
        for (u32 spin = 0; spin < SPIN_MAX * 16; ++spin) {
            if (lds_ld(&sm.cnt[1]) >= NW) break;
            __builtin_amdgcn_s_sleep(GSL2);
        }
        asm volatile("" ::: "memory");
        u32x4 a[2][2];
        const uint16_t* wl = (const uint16_t*)&sm.wo[((tp >> 1) * 8 + r) * 64 + (tp & 1) * 4 + (c >> 3)] + c8;
#pragma unroll
        for (int t = 0; t < 2; ++t)
#pragma unroll
            for (int ks = 0; ks < 2; ++ks)
#pragma unroll
                for (int e = 0; e < 4; ++e) a[t][ks][e] = (u32)wl[(ks * 256 + 2 * e * 8 + t * 2) * 8] | ((u32)wl[(ks * 256 + (2 * e + 1) * 8 + t * 2) * 8] << 16);
        const int red = tp * 4 + (c & 2) + (r >> 1);
        const u64 ny = sm.ep[red < 40 ? red : 0];
        u32 yoff = Y_OFF + ((mat * 80 + ch * 40 + red) * 64 + (YM == 2 ? rh * 32 + h : h * 2 + rh)) * (YM == 1 ? 128 : 64) + ((r & 1) * 2 + (c & 1)) * 16;
        u32 yhi = (u32)(ny >> 32), ylo = (u32)ny;
        asm volatile("" : "+v"(a[0][0]), "+v"(a[0][1]), "+v"(a[1][0]), "+v"(a[1][1]), "+v"(yoff), "+v"(yhi), "+v"(ylo));
        __builtin_amdgcn_s_setprio(3);
        lds_barrier();
        const u32x4 b0 = sm.ov[r], b1 = sm.ov[4 + r];
        f32x4 d0 = {0.f, 0.f, 0.f, 0.f}, d1 = {0.f, 0.f, 0.f, 0.f};
        d0 = __builtin_amdgcn_mfma_f32_16x16x32_bf16(__builtin_bit_cast(bf16x8, a[0][0]), __builtin_bit_cast(bf16x8, b0), d0, 0, 0, 0);
        d1 = __builtin_amdgcn_mfma_f32_16x16x32_bf16(__builtin_bit_cast(bf16x8, a[1][0]), __builtin_bit_cast(bf16x8, b0), d1, 0, 0, 0);
        d0 = __builtin_amdgcn_mfma_f32_16x16x32_bf16(__builtin_bit_cast(bf16x8, a[0][1]), __builtin_bit_cast(bf16x8, b1), d0, 0, 0, 0);
        d1 = __builtin_amdgcn_mfma_f32_16x16x32_bf16(__builtin_bit_cast(bf16x8, a[1][1]), __builtin_bit_cast(bf16x8, b1), d1, 0, 0, 0);
        const f32x4 d = (c & 2) ? d1 : d0;
        const float y0 = (c & 1) ? d[2] : d[0], y1 = (c & 1) ? d[3] : d[1];
        if (c < 4) st_sc1(rws, yoff, u32x4{__float_as_uint(y0), __float_as_uint(y1), yhi, ylo});
    } else {
        lds_barrier();
        if (wave == 11 && j == 0 && lane == 0) __hip_atomic_store((u64*)(ws + G_OFF + h * 256), n1, __ATOMIC_RELAXED, __HIP_MEMORY_SCOPE_AGENT);
    }
    while (__builtin_amdgcn_s_memrealtime() - tsp0 < 3000ull) __builtin_amdgcn_s_sleep(8);
}

extern "C" void kernel_launch(void* const* d_in, const int* in_sizes, int n_in,
                              void* d_out, int out_size, void* d_ws, size_t ws_size,
                              hipStream_t stream) {
    fused<<<NWG, 1024, 0, stream>>>((const uint16_t*)d_in[0], (const uint16_t*)d_in[1], (const uint16_t*)d_in[2], (const uint16_t*)d_in[3], (const uint16_t*)d_in[4],
                                    (const uint16_t*)d_in[5], (const uint16_t*)d_in[6], (const uint16_t*)d_in[7], (const uint16_t*)d_in[8], (const uint16_t*)d_in[9],
                                    (unsigned char*)d_ws, (uint16_t*)d_out);
}
```

```cpp
#include <hip/hip_runtime.h>
#include <hip/hip_bf16.h>
#include <stdint.h>

typedef unsigned int u32;
typedef unsigned long long u64;
typedef __attribute__((ext_vector_type(4))) unsigned int u32x4;
typedef __attribute__((ext_vector_type(2))) unsigned int u32x2;
typedef __attribute__((ext_vector_type(16))) unsigned int u32x16;
typedef __attribute__((ext_vector_type(4))) float f32x4;
typedef __attribute__((ext_vector_type(2))) float f32x2;
typedef __attribute__((ext_vector_type(2))) __bf16 bf16x2;
typedef __attribute__((ext_vector_type(8))) __bf16 bf16x8;
typedef __amdgpu_buffer_rsrc_t rsrc_t;

constexpr int NH = 32, SK = 8192, DH = 128, DM = 640, HD = NH * DH;
constexpr float SCALE = 0.08838834764831845f;
constexpr float LOG2E = 1.4426950408889634f;
constexpr int NWG = 256, NW = 16;
constexpr int NL = 40;
constexpr int NRED = 160;
#define PF 8
#define PFV 6
#define FA1 0
#define FA2 12
#define FA3 24
#define PW 2
#define NPROBE 2
#define GSL2 8
#define DSL 4
#define PCAP 200
#define GSLEEP 1
#define RSLEEP 1
#define WL 1
#define WN 4
#define WP0 6
#define TL 1
#define XA 4
#define QSL 4
#define SSL 4
constexpr u32 SPOLL = 64;
constexpr u32 SPIN_MAX = 1u << 20;
constexpr u32 E_OFF = 0;
constexpr u32 G_OFF = 2048;
constexpr u32 Q_OFF = 16384;
constexpr u32 R_OFF = Q_OFF + 32 * 1024;
constexpr u32 Y_OFF = R_OFF + 2 * 32 * 2 * 4096;
constexpr u32 D_OFF = Y_OFF + NRED * 64 * 64;
constexpr u32 S_OFF = D_OFF + 32 * 128;
constexpr u32 WS_BYTES = S_OFF + 32 * 8 * 16 * 256;

__device__ __forceinline__ float dot2(u32 a, u32 b, float c) {
    return __builtin_amdgcn_fdot2_f32_bf16(__builtin_bit_cast(bf16x2, a), __builtin_bit_cast(bf16x2, b), c, false);
}
template <int CTRL> __device__ __forceinline__ float dpp(float x) {
    return __builtin_bit_cast(float, __builtin_amdgcn_mov_dpp(__builtin_bit_cast(int, x), CTRL, 0xf, 0xf, true));
}
constexpr int XOR1 = 0xB1, XOR2 = 0x4E, HMIR = 0x141, MIR = 0x140, ROR4 = 0x124, ROR8 = 0x128;
__device__ __forceinline__ float row16_sum(float x) {
    x += dpp<XOR1>(x); x += dpp<XOR2>(x); x += dpp<HMIR>(x); x += dpp<MIR>(x); return x;
}
__device__ __forceinline__ float row16_max(float x) {
    x = fmaxf(x, dpp<XOR1>(x)); x = fmaxf(x, dpp<XOR2>(x)); x = fmaxf(x, dpp<HMIR>(x)); x = fmaxf(x, dpp<MIR>(x)); return x;
}
__device__ __forceinline__ float sw16_sum2(float a, float b) {
    auto s = __builtin_amdgcn_permlane16_swap(__float_as_uint(a), __float_as_uint(b), false, false);
    return __uint_as_float(s[0]) + __uint_as_float(s[1]);
}
__device__ __forceinline__ float sw32_sum2(float a, float b) {
    auto t = __builtin_amdgcn_permlane32_swap(__float_as_uint(a), __float_as_uint(b), false, false);
    return __uint_as_float(t[0]) + __uint_as_float(t[1]);
}
__device__ __forceinline__ float xrow_sum(float x) { x = sw16_sum2(x, x); return sw32_sum2(x, x); }
__device__ __forceinline__ float sw32_max(float x) {
    auto t = __builtin_amdgcn_permlane32_swap(__float_as_uint(x), __float_as_uint(x), false, false);
    return fmaxf(__uint_as_float(t[0]), __uint_as_float(t[1]));
}
__device__ __forceinline__ float xrow_max(float x) {
    auto s = __builtin_amdgcn_permlane16_swap(__float_as_uint(x), __float_as_uint(x), false, false);
    return sw32_max(fmaxf(__uint_as_float(s[0]), __uint_as_float(s[1])));
}
__device__ __forceinline__ float rdlane(float x, int l) { return __builtin_bit_cast(float, __builtin_amdgcn_readlane(__builtin_bit_cast(int, x), l)); }
__device__ __forceinline__ float bf16r(float x) { return (float)(__bf16)x; }
__device__ __forceinline__ float lo16(u32 w) { return __uint_as_float(w << 16); }
__device__ __forceinline__ float hi16(u32 w) { return __uint_as_float(w & 0xffff0000u); }
__device__ __forceinline__ rsrc_t mkrsrc(const void* p, u32 bytes) { return __builtin_amdgcn_make_buffer_rsrc((void*)p, 0, bytes, 0x00020000); }
template <int AUX> __device__ __forceinline__ u32x4 bld(rsrc_t r, u32 off, u32 soff = 0) { return __builtin_amdgcn_raw_buffer_load_b128(r, off, soff, AUX); }
__device__ __forceinline__ u32x4 ld_sc1(rsrc_t r, u32 off, u32 soff = 0) { return __builtin_amdgcn_raw_buffer_load_b128(r, off, soff, 16); }
__device__ __forceinline__ void st_sc1(rsrc_t r, u32 off, u32x4 v) { __builtin_amdgcn_raw_buffer_store_b128(v, r, off, 0, 16); }
__device__ __forceinline__ void lds_barrier() { asm volatile("s_waitcnt lgkmcnt(0)\n\ts_barrier" ::: "memory"); }
__device__ __forceinline__ void lds_wait() { asm volatile("s_waitcnt lgkmcnt(0)" ::: "memory"); }
template <int N> __device__ __forceinline__ void vm_wait() { __builtin_amdgcn_s_waitcnt(0x0f70 | (N & 15) | ((N >> 4) << 14)); }
__device__ __forceinline__ u64 uni64(u64 v) { return ((u64)(u32)__builtin_amdgcn_readfirstlane((u32)(v >> 32)) << 32) | (u32)__builtin_amdgcn_readfirstlane((u32)v); }

__device__ __forceinline__ u64 sld64(const void* p) { u64 v; asm volatile("s_load_dwordx2 %0, %1, 0x0 glc\n\ts_waitcnt lgkmcnt(0)" : "=&s"(v) : "s"(p) : "memory"); return v; }
__device__ __forceinline__ u32x16 sld16(const void* p) { u32x16 v; asm volatile("s_load_dwordx16 %0, %1, 0x0 glc\n\ts_waitcnt lgkmcnt(0)" : "=&s"(v) : "s"(p) : "memory"); return v; }
__device__ __forceinline__ u32 tbad(const u32x16& d, u32 hi, u32 lo) {
    return ((d[2] ^ hi) | (d[3] ^ lo)) | ((d[6] ^ hi) | (d[7] ^ lo)) | ((d[10] ^ hi) | (d[11] ^ lo)) | ((d[14] ^ hi) | (d[15] ^ lo));
}
__device__ __forceinline__ u32 wrlane(u32 v, int l, u32 old) { asm("v_writelane_b32 %0, %1, %2" : "+v"(old) : "s"(v), "n"(l)); return old; }
__device__ __forceinline__ void glds16(const void* gsrc, u32 lds_dst) {
    u32 keep;
    asm volatile("s_mov_b32 %0, m0\n\ts_mov_b32 m0, %2\n\ts_nop 0\n\tglobal_load_lds_dwordx4 %1, off nt\n\ts_mov_b32 m0, %0" : "=&s"(keep) : "v"(gsrc), "s"(lds_dst) : "memory");
}
__device__ __forceinline__ u32 lds_ld(const u32* p) { return __builtin_amdgcn_readfirstlane(__hip_atomic_load(p, __ATOMIC_RELAXED, __HIP_MEMORY_SCOPE_WORKGROUP)); }
__device__ __forceinline__ u32 bad48(u32x4 g, u32 hi, u32 lo) { return (((g[0] ^ hi) | (g[1] ^ (hi >> 8))) & 255u) | (g[3] ^ lo); }
__device__ __forceinline__ u32 bad64(u32x4 g, u32 hi, u32 lo) { return (g[2] ^ hi) | (g[3] ^ lo); }

struct Smem {
    u32x4 wo[40 * 64];
    float part[2][NW][128];
    float ml[2][NW][2];
    u32x4 ov[8];
    float qp[4][32];
    u64 ep[48], n1;
    u32 qv[128];
    u32 qcnt, q4, cnt[2], dec, pdone, wdone;
};

__device__ __forceinline__ void combine_publish(Smem& sm, int ab, int lane, rsrc_t rws, int h, int slot, u32 nhi, u32 nlo) {
    const f32x2 ml = *(const f32x2*)&sm.ml[ab][lane & 15][0];
    f32x2 pv[NW];
#pragma unroll
    for (int w = 0; w < NW; ++w) pv[w] = *(const f32x2*)&sm.part[ab][w][2 * lane];
    const u32 off = R_OFF + (((ab * 32 + h) * 2 + (lane >> 5)) * 8 + slot) * 512 + (lane & 31) * 16;
    __builtin_amdgcn_sched_barrier(0);
    const float M = row16_max(ml[0]);
    const float f = __builtin_amdgcn_exp2f(ml[0] - M);
    const float L = row16_sum(f * ml[1]);
    f32x2 o[4];
#pragma unroll
    for (int w = 0; w < NW; ++w) { if (w < 4) o[w] = rdlane(f, w) * pv[w]; else o[w & 3] += rdlane(f, w) * pv[w]; }
    const f32x2 t = ((o[0] + o[1]) + (o[2] + o[3])) * __builtin_amdgcn_rcpf(L);
    const float W = M + __builtin_amdgcn_logf(L);
    st_sc1(rws, off, u32x4{(__float_as_uint(t[0]) & ~255u) | (nhi & 255u), (__float_as_uint(t[1]) & ~255u) | ((nhi >> 8) & 255u), __float_as_uint(W), nlo});
}

__global__ void __launch_bounds__(1024) fused(const uint16_t* __restrict__ xA, const uint16_t* __restrict__ kA, const uint16_t* __restrict__ vA,
                                              const uint16_t* __restrict__ wqA, const uint16_t* __restrict__ woA,
                                              const uint16_t* __restrict__ xB, const uint16_t* __restrict__ kB, const uint16_t* __restrict__ vB,
                                              const uint16_t* __restrict__ wqB, const uint16_t* __restrict__ woB,
                                              unsigned char* __restrict__ ws, uint16_t* __restrict__ out) {
    __shared__ __attribute__((aligned(16))) Smem sm;
    const int b = blockIdx.x;
    const int h = b >> 3, j = b & 7;
    const int tid = threadIdx.x, lane = tid & 63, wave = __builtin_amdgcn_readfirstlane(tid >> 6), r = lane >> 4, c = lane & 15;
    const int row8 = lane >> 3, c8 = lane & 7;
    const int mat = j >> 2, rh = (j >> 1) & 1, ch = j & 1;
    const rsrc_t rws = mkrsrc(ws, WS_BYTES);
    const rsrc_t rkA = mkrsrc(kA, NH * SK * DH * 2), rvA = mkrsrc(vA, NH * SK * DH * 2);
    const rsrc_t rkB = mkrsrc(kB, NH * SK * DH * 2), rvB = mkrsrc(vB, NH * SK * DH * 2);
    const u32 lo = lane * 16;
#define CLS(k) ((u32)((((k) & 1) << 14) | (((k) & 2) << 11) | (((k) & 4) << 11)))
    const u32 soB = (u32)h * (SK * 256) + ((48 + wave) << 15) + CLS(j ^ (wave & 7));
    u32x4 buf[NL];
#define GX(i) ((u32)(((i) << 19) | (((i) & 1) << 14) | (((i) >> 1) << 12)))
#define LD(n) ((n) < 8 ? bld<2>(((n) & 4) ? rvB : rkB, lo + ((n) & 3) * 1024, soB) : (n) < 24 ? bld<2>(rkA, lo + ((n) & 3) * 1024, soK ^ GX(((n) - 8) >> 2)) : bld<2>(rvA, lo + ((n) & 3) * 1024, soV ^ GX(((n) - 24) >> 2)))
    __builtin_amdgcn_s_setprio(3);
    u32 soK = 0, soV = 0;
    if (wave < 4) {
        const int hq = (h & ~1) | (j >> 2), qm = (j >> 1) & 1, qd0 = (j & 1) * 64 + (h & 1) * 32;
        const rsrc_t rw = mkrsrc(qm ? wqB : wqA, DM * HD * 2), rx = mkrsrc(qm ? xB : xA, DM * 2);
        const u32 wb = (wave * 160 * HD + hq * DH + qd0) * 2;
        const u32x4 x4 = bld<0>(rx, 320 * wave + 20 * (lane >> 2));
        const u32 x1 = __builtin_amdgcn_raw_buffer_load_b32(rx, 320 * wave + 20 * (lane >> 2) + 16, 0, 0);
        u32x4 w[10];
#pragma unroll
        for (int i = 0; i < 10; ++i) w[i] = bld<2>(rw, ((lane >> 2) * 10 * HD + (lane & 3) * 8) * 2, wb + i * HD * 2);
        __builtin_amdgcn_sched_barrier(0);
        lds_barrier();
        const u64 nq = sld64(ws + G_OFF + hq * 256) + 1;
        __builtin_amdgcn_sched_barrier(0);
        float q[8];
#pragma unroll
        for (int k = 0; k < 8; ++k) q[k] = 0.f;
#pragma unroll
        for (int i = 0; i < 10; ++i) {
            const u32 xw = i < 8 ? x4[i >> 1] : x1;
            const float xs = (i & 1) ? hi16(xw) : lo16(xw);
#pragma unroll
            for (int k = 0; k < 4; ++k) { q[2 * k] = fmaf(xs, lo16(w[i][k]), q[2 * k]); q[2 * k + 1] = fmaf(xs, hi16(w[i][k]), q[2 * k + 1]); }
        }
#pragma unroll
        for (int k = 0; k < 8; ++k) { q[k] += dpp<ROR4>(q[k]); q[k] += dpp<ROR8>(q[k]); q[k] = xrow_sum(q[k]); }
        if (lane < 4) { *(f32x4*)&sm.qp[wave][8 * lane] = f32x4{q[0], q[1], q[2], q[3]}; *(f32x4*)&sm.qp[wave][8 * lane + 4] = f32x4{q[4], q[5], q[6], q[7]}; }
        u32 old = 0;
        lds_wait();
        if (lane == 0) old = __hip_atomic_fetch_add(&sm.q4, 1u, __ATOMIC_RELAXED, __HIP_MEMORY_SCOPE_WORKGROUP);
        if (__builtin_amdgcn_readfirstlane(old) == 3) {
            asm volatile("" ::: "memory");
            const f32x4 s = (*(const f32x4*)&sm.qp[0][4 * c8] + *(const f32x4*)&sm.qp[1][4 * c8]) + (*(const f32x4*)&sm.qp[2][4 * c8] + *(const f32x4*)&sm.qp[3][4 * c8]);
            const u32 p0 = (__float_as_uint(bf16r(s[0])) >> 16) | (__float_as_uint(bf16r(s[1])) & 0xffff0000u);
            const u32 p1 = (__float_as_uint(bf16r(s[2])) >> 16) | (__float_as_uint(bf16r(s[3])) & 0xffff0000u);
            if (lane < 8) st_sc1(rws, Q_OFF + hq * 1024 + (qm * 128 + qd0) * 4 + lo, u32x4{p0, p1, (u32)(nq >> 32), (u32)nq});
        }
#pragma nounroll
        for (u32 spin = 0; spin < SPIN_MAX * 16; ++spin) {
            if (lds_ld(&sm.pdone)) break;
            __builtin_amdgcn_s_sleep(2);
        }
        asm volatile("" ::: "memory");
        {
            const u64 n1 = uni64(sm.n1);
            const u32 nhi = (u32)(n1 >> 32), nlo = (u32)n1;
            const u32 qb = __builtin_amdgcn_readfirstlane(Q_OFF + h * 1024 + wave * 256);
            bool ok = false;
            u32x16 d0, d1, d2, d3;
#pragma nounroll
            for (u32 spin = 0; spin < SPOLL; ++spin) {
                asm volatile("s_load_dwordx16 %0, %4, 0x0 glc\n\ts_load_dwordx16 %1, %4, 0x40 glc\n\ts_load_dwordx16 %2, %4, 0x80 glc\n\ts_load_dwordx16 %3, %4, 0xc0 glc\n\ts_waitcnt lgkmcnt(0)"
                             : "=&s"(d0), "=&s"(d1), "=&s"(d2), "=&s"(d3) : "s"(ws + qb) : "memory");
                if (((tbad(d0, nhi, nlo) | tbad(d1, nhi, nlo)) | (tbad(d2, nhi, nlo) | tbad(d3, nhi, nlo))) == 0) { ok = true; break; }
                __builtin_amdgcn_s_sleep(QSL);
            }
            u32 g0 = 0, g1 = 0;
            if (ok) {
#pragma unroll
                for (int i = 0; i < 4; ++i) {
                    g0 = wrlane(d0[4 * i], i, g0); g1 = wrlane(d0[4 * i + 1], i, g1);
                    g0 = wrlane(d1[4 * i], 4 + i, g0); g1 = wrlane(d1[4 * i + 1], 4 + i, g1);
                    g0 = wrlane(d2[4 * i], 8 + i, g0); g1 = wrlane(d2[4 * i + 1], 8 + i, g1);
                    g0 = wrlane(d3[4 * i], 12 + i, g0); g1 = wrlane(d3[4 * i + 1], 12 + i, g1);
                }
            } else {
                u32 off = qb + c * 16;
                u32x4 g;
#pragma nounroll
                for (u32 spin = 0; spin < SPIN_MAX; ++spin) {
                    asm volatile("" : "+v"(off));
                    g = ld_sc1(rws, off);
                    if (__all(bad64(g, nhi, nlo) == 0)) break;
                    __builtin_amdgcn_s_sleep(1);
                }
                g0 = g[0]; g1 = g[1];
            }
            if (lane < 16) *(u32x2*)&sm.qv[wave * 32 + 2 * lane] = u32x2{g0, g1};
            lds_wait();
            if (lane == 0) __hip_atomic_fetch_add(&sm.qcnt, 1u, __ATOMIC_RELAXED, __HIP_MEMORY_SCOPE_WORKGROUP);
        }
        __builtin_amdgcn_sched_barrier(0);
#pragma unroll
        for (int n = 0; n < 8; ++n) {
            if (n >= PW && lds_ld(&sm.dec) == 0) vm_wait<PW - 1>();
            buf[n] = LD(n);
            __builtin_amdgcn_sched_barrier(0);
        }
    } else if (wave == 12) {
        if (lane == 0) { sm.qcnt = 0; sm.q4 = 0; sm.cnt[0] = 0; sm.cnt[1] = 0; sm.dec = 0; sm.pdone = 0; sm.wdone = 0; }
        lds_barrier();
        int gs = 0;
        {
            const unsigned char* pa = (const unsigned char*)((j & 1) ? vA : kA) + (size_t)h * (SK * 256) + (j >> 1) * (16 * 32768);
#pragma unroll
            for (int i = 0; i < NPROBE; ++i) {
                const int side = ((j >> 1) ^ i) & 1;
                const unsigned char* p1 = pa + i * 32768 + side * 8192;
                const unsigned char* p2 = pa + i * 32768 + (side ^ 1) * 8192;
                u32 v1, v2; u64 t1, t2;
                asm volatile("s_load_dword %0, %4, 0x0 glc\n\ts_load_dword %1, %5, 0x0 glc\n\ts_waitcnt lgkmcnt(1)\n\ts_memtime %2\n\ts_waitcnt lgkmcnt(0)\n\ts_memtime %3\n\ts_waitcnt lgkmcnt(0)"
                             : "=&s"(v1), "=&s"(v2), "=&s"(t1), "=&s"(t2) : "s"(p1), "s"(p2) : "memory");
                const int g = min((int)(u32)(t2 - t1), PCAP);
                gs += side ? -g : g;
            }
        }
        u64 e1;
        {
            u32 dummy;
            asm volatile("s_load_dword %1, %3, 0x0\n\ts_load_dwordx2 %0, %2, 0x0 glc\n\ts_waitcnt lgkmcnt(0)" : "=&s"(e1), "=&s"(dummy)
                         : "s"(ws + G_OFF + h * 256), "s"((const unsigned char*)((j & 1) ? kA : vA) + (size_t)h * (SK * 256) + j * 4096) : "memory");
        }
        const u64 n1 = e1 + 1;
        const u32 nhi = (u32)(n1 >> 32), nlo = (u32)n1;
        const int iod0 = (__builtin_amdgcn_s_getreg((20 << 0) | (0 << 6) | (3 << 11)) >> 2) & 1;
        if (lane == 0) st_sc1(rws, D_OFF + h * 128 + j * 16, u32x4{(u32)(iod0 ? -gs : gs), (u32)iod0, nhi, nlo});
        if (lane == 0) { sm.n1 = n1; lds_wait(); __hip_atomic_store(&sm.pdone, 1u, __ATOMIC_RELAXED, __HIP_MEMORY_SCOPE_WORKGROUP); }
        int vg[8], vi[8];
        bool ok = false;
#pragma nounroll
        for (u32 spin = 0; spin < SPOLL; ++spin) {
            __builtin_amdgcn_s_sleep(2);
            u32x16 d0, d1;
            asm volatile("s_load_dwordx16 %0, %2, 0x0 glc\n\ts_load_dwordx16 %1, %2, 0x40 glc\n\ts_waitcnt lgkmcnt(0)" : "=&s"(d0), "=&s"(d1) : "s"(ws + (u32)__builtin_amdgcn_readfirstlane(D_OFF + h * 128)) : "memory");
#pragma unroll
            for (int i = 0; i < 4; ++i) { vg[i] = d0[4 * i]; vi[i] = d0[4 * i + 1]; vg[4 + i] = d1[4 * i]; vi[4 + i] = d1[4 * i + 1]; }
            if ((tbad(d0, nhi, nlo) | tbad(d1, nhi, nlo)) == 0) { ok = true; break; }
        }
        if (!ok) {
            u32 off = D_OFF + h * 128 + c8 * 16;
            u32x4 g;
#pragma nounroll
            for (u32 spin = 0; spin < SPIN_MAX; ++spin) {
                asm volatile("" : "+v"(off));
                g = ld_sc1(rws, off);
                if (__all(bad64(g, nhi, nlo) == 0)) break;
                __builtin_amdgcn_s_sleep(1);
            }
#pragma unroll
            for (int i = 0; i < 8; ++i) { vg[i] = __builtin_amdgcn_readlane(g[0], i); vi[i] = __builtin_amdgcn_readlane(g[1], i); }
        }
        int msk = 0;
#pragma unroll
        for (int i = 0; i < 8; ++i) msk |= (vi[i] & 1) << i;
        const int sk = (vg[0] + vg[2]) + (vg[4] + vg[6]), sv = (vg[1] + vg[3]) + (vg[5] + vg[7]);
        const int iod = (msk >> j) & 1;
        const int rank = __builtin_popcount((iod ? msk : ~msk) & ((1 << j) - 1));
        const int pk_ = sk > 0 ? 0 : 1, pv_ = sv > 0 ? 0 : 1;
        const bool bal = __builtin_popcount(msk) == 4;
        const u32 d = 0x100u | (bal ? ((iod ^ pk_) * 4 + rank) | (((iod ^ pv_) * 4 + rank) << 4) : j * 17);
        if (lane == 0) __hip_atomic_store(&sm.dec, d, __ATOMIC_RELAXED, __HIP_MEMORY_SCOPE_WORKGROUP);
        __builtin_amdgcn_sched_barrier(0);
#pragma unroll
        for (int n = 0; n < 8; ++n) { buf[n] = LD(n); __builtin_amdgcn_sched_barrier(0); }
    } else {
        lds_barrier();
        if (!WL && wave < 12) {
            const uint16_t* wsrc = (mat ? woB : woA) + (size_t)(h * DH + rh * 64 + row8) * DM + ch * 320 + c8 * 8;
#pragma unroll
            for (int i = 0; i < 5; ++i) {
                const int idx = (wave - 4) * 5 + i;
                if (i >= PW) vm_wait<PW - 1>();
                glds16(wsrc + (idx & 7) * 8 * DM + (idx >> 3) * 64, (u32)(uintptr_t)&sm.wo[idx * 64]);
            }
        }
        __builtin_amdgcn_sched_barrier(0);
        const u64 ev = __hip_atomic_load((const u64*)(ws + E_OFF) + (lane < 40 ? mat * 80 + ch * 40 + lane : b < NRED ? b : 0), __ATOMIC_RELAXED, __HIP_MEMORY_SCOPE_AGENT);
#pragma unroll
        for (int n = 0; n < 8; ++n) {
            if (lds_ld(&sm.dec) == 0) vm_wait<PW - 1>();
            buf[n] = LD(n);
            __builtin_amdgcn_sched_barrier(0);
        }
        if (wave == 8 && lane < 48) sm.ep[lane] = ev + 1;
    }
    __builtin_amdgcn_sched_barrier(0);
    u32 dec = 0;
#pragma nounroll
    for (u32 spin = 0; spin < SPIN_MAX * 16; ++spin) {
        dec = lds_ld(&sm.dec);
        if (dec) break;
        __builtin_amdgcn_s_sleep(DSL);
    }
    const int kK = dec & 7, kV = (dec >> 4) & 7;
    soK = (u32)h * (SK * 256) + (wave << 15) + CLS(kK);
    soV = (u32)h * (SK * 256) + (wave << 15) + CLS(kV);
    __builtin_amdgcn_sched_barrier(0);
#pragma unroll
    for (int n = 8; n < 8 + PF; ++n) { buf[n] = LD(n); __builtin_amdgcn_sched_barrier(0); }
    __builtin_amdgcn_sched_barrier(0);
#pragma nounroll
    for (u32 spin = 0; spin < SPIN_MAX * 16; ++spin) {
        if (lds_ld(&sm.qcnt) == 4) break;
        __builtin_amdgcn_s_sleep(DSL);
    }
    asm volatile("" ::: "memory");
    const u32x4 qa4 = *(const u32x4*)&sm.qv[4 * c], qb4 = *(const u32x4*)&sm.qv[64 + 4 * c];
    const u64 n1 = uni64(sm.n1);
    const u32 nhi = (u32)(n1 >> 32), nlo = (u32)n1;
    float acc[8], m = -INFINITY, l = 0.f, sc[4];
#pragma unroll
    for (int k = 0; k < 8; ++k) acc[k] = 0.f;
#define FLUSH(AB) do { const float t0_ = sw32_sum2(acc[0], acc[4]), t1_ = sw32_sum2(acc[1], acc[5]), t2_ = sw32_sum2(acc[2], acc[6]), t3_ = sw32_sum2(acc[3], acc[7]); \
        *(f32x2*)&sm.part[AB][wave][8 * c + 2 * r] = f32x2{sw16_sum2(t0_, t2_), sw16_sum2(t1_, t3_)}; } while (0)
#define PUTML(AB) do { l = xrow_sum(l); if (lane == 0) *(f32x2*)&sm.ml[AB][wave][0] = f32x2{m, l}; } while (0)
#define TS(n) ((n) < 8 ? (n) + 16 : (n) < 24 ? (n) - 8 : (n))
#define NEXT(n) do { __builtin_amdgcn_sched_barrier(0); \
        if (TS(n) == FA1) __builtin_amdgcn_s_setprio(2); if (TS(n) == FA2) __builtin_amdgcn_s_setprio(1); if (TS(n) == FA3) __builtin_amdgcn_s_setprio(0); \
        if ((n) >= 8 && (n) + PF < 24) buf[(n) + PF] = LD((n) + PF); \
        if (TS(n) + PFV >= 24 && TS(n) + PFV < NL) buf[TS(n) + PFV] = LD(TS(n) + PFV); \
        __builtin_amdgcn_sched_barrier(0); } while (0)
#define SCORE(B, Q) ({ float d_ = dot2((B)[0], (Q)[0], 0.f); d_ = dot2((B)[1], (Q)[1], d_); d_ = dot2((B)[2], (Q)[2], d_); d_ = dot2((B)[3], (Q)[3], d_); \
                       d_ = row16_sum(d_); bf16r(bf16r(d_) * SCALE); })
#define PV(B, P) do { _Pragma("unroll") for (int k = 0; k < 4; ++k) { acc[2 * k] = fmaf((P), lo16((B)[k]), acc[2 * k]); acc[2 * k + 1] = fmaf((P), hi16((B)[k]), acc[2 * k + 1]); } \
        asm volatile("" : "+v"(acc[0]), "+v"(acc[1]), "+v"(acc[2]), "+v"(acc[3]), "+v"(acc[4]), "+v"(acc[5]), "+v"(acc[6]), "+v"(acc[7]) :: "memory"); } while (0)
    const bool xch = kK != kV;
    u32 pk[8];
#pragma unroll
    for (int n = 8; n < 24; ++n) {
        const u32 sb = __float_as_uint(SCORE(buf[n], qa4));
        if (n & 1) pk[(n - 8) >> 1] |= sb & 0xffff0000u; else pk[(n - 8) >> 1] = sb >> 16;
        asm volatile("" : "+v"(pk[(n - 8) >> 1]) :: "memory");
        if (n == 23 && xch) {
            u32 w0 = pk[0], w1 = pk[1];
#pragma unroll
            for (int k = 1; k < 4; ++k) { w0 = c == k ? pk[2 * k] : w0; w1 = c == k ? pk[2 * k + 1] : w1; }
            if (c < 4) st_sc1(rws, S_OFF + ((h * 8 + kK) * 16 + wave) * 256 + c * 64 + r * 16, u32x4{w0, w1, nhi, nlo});
        }
        NEXT(n);
    }
#pragma unroll
    for (int n = 0; n < 4; ++n) { sc[n] = SCORE(buf[n], qb4) * LOG2E; asm volatile("" : "+v"(sc[n]) :: "memory"); NEXT(n); }
    m = xrow_max(fmaxf(fmaxf(sc[0], sc[1]), fmaxf(sc[2], sc[3])));
#pragma unroll
    for (int n = 4; n < 8; ++n) { const float p = __builtin_amdgcn_exp2f(sc[n & 3] - m); l += p; PV(buf[n], p); NEXT(n); }
    {
        PUTML(1);
        FLUSH(1);
        u32 old = 0;
        lds_wait();
        if (lane == 0) old = __hip_atomic_fetch_add(&sm.cnt[1], 1u, __ATOMIC_RELAXED, __HIP_MEMORY_SCOPE_WORKGROUP);
        if (__builtin_amdgcn_readfirstlane(old) == NW - 1) { asm volatile("" ::: "memory"); combine_publish(sm, 1, lane, rws, h, j, nhi, nlo); }
        l = 0.f;
#pragma unroll
        for (int k = 0; k < 8; ++k) acc[k] = 0.f;
        __builtin_amdgcn_sched_barrier(0);
    }
    const u32 sb = __builtin_amdgcn_readfirstlane(S_OFF + ((h * 8 + kV) * 16 + wave) * 256);
#define VPHASE(I0) do { \
        m = xrow_max(fmaxf(fmaxf(fmaxf(fmaxf(lo16(pk[0]), hi16(pk[0])), fmaxf(lo16(pk[1]), hi16(pk[1]))), fmaxf(fmaxf(lo16(pk[2]), hi16(pk[2])), fmaxf(lo16(pk[3]), hi16(pk[3])))), \
                           fmaxf(fmaxf(fmaxf(lo16(pk[4]), hi16(pk[4])), fmaxf(lo16(pk[5]), hi16(pk[5]))), fmaxf(fmaxf(lo16(pk[6]), hi16(pk[6])), fmaxf(lo16(pk[7]), hi16(pk[7])))))) * LOG2E; \
        _Pragma("unroll") for (int i = 0; i < 4; ++i) { \
            const u32 s0 = pk[2 * i], s1 = pk[2 * i + 1]; \
            sc[0] = __builtin_amdgcn_exp2f(lo16(s0) * LOG2E - m); sc[1] = __builtin_amdgcn_exp2f(hi16(s0) * LOG2E - m); \
            sc[2] = __builtin_amdgcn_exp2f(lo16(s1) * LOG2E - m); sc[3] = __builtin_amdgcn_exp2f(hi16(s1) * LOG2E - m); \
            l += (sc[0] + sc[1]) + (sc[2] + sc[3]); \
            if (i == 3) PUTML(0); \
            __builtin_amdgcn_sched_barrier(0); \
            _Pragma("unroll") for (int n = 24 + 4 * i; n < 28 + 4 * i; ++n) { PV(buf[n], sc[n & 3]); __builtin_amdgcn_sched_barrier(0); \
                if (n == FA3) __builtin_amdgcn_s_setprio(0); \
                if (n + PFV >= (I0) && n + PFV < NL) buf[n + PFV] = LD(n + PFV); \
                __builtin_amdgcn_sched_barrier(0); } \
        } \
        FLUSH(0); } while (0)
#define SPREAD(D) do { _Pragma("unroll") for (int i = 0; i < 4; ++i) { \
            _Pragma("unroll") for (int k = 0; k < 4; ++k) { pk[2 * i] = wrlane((D)[i][4 * k], 16 * k, pk[2 * i]); pk[2 * i + 1] = wrlane((D)[i][4 * k + 1], 16 * k, pk[2 * i + 1]); } } \
        asm volatile("s_nop 1" : "+v"(pk[6]), "+v"(pk[7])); \
        _Pragma("unroll") for (int k = 0; k < 8; ++k) pk[k] = __builtin_amdgcn_mov_dpp(pk[k], 0x150, 0xf, 0xf, true); } while (0)
#define SLD4(D) asm volatile("s_load_dwordx16 %0, %4, 0x0 glc\n\ts_load_dwordx16 %1, %4, 0x40 glc\n\ts_load_dwordx16 %2, %4, 0x80 glc\n\ts_load_dwordx16 %3, %4, 0xc0 glc\n\ts_waitcnt lgkmcnt(0)" \
                     : "=&s"((D)[0]), "=&s"((D)[1]), "=&s"((D)[2]), "=&s"((D)[3]) : "s"(ws + sb) : "memory")
#define TB4(D) ((tbad((D)[0], nhi, nlo) | tbad((D)[1], nhi, nlo)) | (tbad((D)[2], nhi, nlo) | tbad((D)[3], nhi, nlo)))
    bool late = false;
    if (xch) {
        u32x16 d[4];
        SLD4(d);
        late = TB4(d) != 0;
        if (!late) SPREAD(d);
    }
    if (late) {
#pragma unroll
        for (int n = 24 + PFV; n < 24 + PFV + XA; ++n) { buf[n] = LD(n); __builtin_amdgcn_sched_barrier(0); }
        bool ok = false;
        u32x16 d[4];
#pragma nounroll
        for (u32 spin = 0; spin < SPOLL; ++spin) {
            SLD4(d);
            if (TB4(d) == 0) { ok = true; break; }
            __builtin_amdgcn_s_sleep(SSL);
        }
        if (ok) SPREAD(d);
        else {
            u32 soff = sb + (c & 3) * 64 + r * 16;
            u32x4 g;
#pragma nounroll
            for (u32 spin = 0; spin < SPIN_MAX; ++spin) {
                asm volatile("" : "+v"(soff));
                g = ld_sc1(rws, soff);
                if (__all(bad64(g, nhi, nlo) == 0)) break;
                __builtin_amdgcn_s_sleep(1);
            }
            pk[0] = __builtin_amdgcn_mov_dpp(g[0], 0x150, 0xf, 0xf, true); pk[1] = __builtin_amdgcn_mov_dpp(g[1], 0x150, 0xf, 0xf, true);
            pk[2] = __builtin_amdgcn_mov_dpp(g[0], 0x151, 0xf, 0xf, true); pk[3] = __builtin_amdgcn_mov_dpp(g[1], 0x151, 0xf, 0xf, true);
            pk[4] = __builtin_amdgcn_mov_dpp(g[0], 0x152, 0xf, 0xf, true); pk[5] = __builtin_amdgcn_mov_dpp(g[1], 0x152, 0xf, 0xf, true);
            pk[6] = __builtin_amdgcn_mov_dpp(g[0], 0x153, 0xf, 0xf, true); pk[7] = __builtin_amdgcn_mov_dpp(g[1], 0x153, 0xf, 0xf, true);
        }
        if (XA) VPHASE(24 + PFV + XA);
    }
    if (!late || !XA) VPHASE(24 + PFV);
    {
        u32 old = 0;
        lds_wait();
        if (lane == 0) old = __hip_atomic_fetch_add(&sm.cnt[0], wave == 5 ? 1u : 0x101u, __ATOMIC_RELAXED, __HIP_MEMORY_SCOPE_WORKGROUP);
        old = __builtin_amdgcn_readfirstlane(old);
        if ((old & 255) == NW - 1) {
            asm volatile("" ::: "memory"); __builtin_amdgcn_s_setprio(3); combine_publish(sm, 0, lane, rws, h, kV, nhi, nlo);
        }
        const u32 pos = (old >> 8) - WP0;
        if (WL && wave != 5 && pos < WN) {
            const uint16_t* wsrc = (mat ? woB : woA) + (size_t)(h * DH + rh * 64 + row8) * DM + ch * 320 + c8 * 8;
#pragma unroll
            for (int i = 0; i < 40 / WN; ++i) {
                const int idx = pos * (40 / WN) + i;
                glds16(wsrc + (idx & 7) * 8 * DM + (idx >> 3) * 64, (u32)(uintptr_t)&sm.wo[idx * 64]);
            }
            vm_wait<0>();
            if (lane == 0) __hip_atomic_fetch_add(&sm.wdone, 1u, __ATOMIC_RELAXED, __HIP_MEMORY_SCOPE_WORKGROUP);
        }
    }
    if (wave == 5) {
#pragma nounroll
        for (u32 spin = 0; spin < SPIN_MAX * 16; ++spin) {
            if ((lds_ld(&sm.cnt[mat]) & 255) >= NW) break;
            __builtin_amdgcn_s_sleep(GSL2);
        }
        __builtin_amdgcn_s_setprio(3);
        u32 off = R_OFF + ((mat * 32 + h) * 2 + rh) * 4096 + lo;
        u32x4 g[4];
#pragma unroll
        for (int i = 0; i < 4; ++i) g[i] = ld_sc1(rws, off + i * 1024);
#pragma nounroll
        for (u32 spin = 0; spin < SPIN_MAX; ++spin) {
            bool p[4];
#pragma unroll
            for (int i = 0; i < 4; ++i) p[i] = bad48(g[i], nhi, nlo) != 0;
            if (!__any(p[0] | p[1] | p[2] | p[3])) break;
            if (GSLEEP) __builtin_amdgcn_s_sleep(GSLEEP);
            asm volatile("" : "+v"(off));
#pragma unroll
            for (int i = 0; i < 4; ++i) if (p[i]) g[i] = ld_sc1(rws, off + i * 1024);
        }
        const float W = sw32_max(fmaxf(fmaxf(__uint_as_float(g[0][2]), __uint_as_float(g[1][2])), fmaxf(__uint_as_float(g[2][2]), __uint_as_float(g[3][2]))));
        float o0 = 0.f, o1 = 0.f, S = 0.f;
#pragma unroll
        for (int i = 0; i < 4; ++i) {
            const float e = __builtin_amdgcn_exp2f(__uint_as_float(g[i][2]) - W);
            S += e; o0 = fmaf(e, __uint_as_float(g[i][0] & ~255u), o0); o1 = fmaf(e, __uint_as_float(g[i][1] & ~255u), o1);
        }
        const float o = sw32_sum2(o0, o1) * __builtin_amdgcn_rcpf(sw32_sum2(S, S));
        ((uint16_t*)sm.ov)[2 * (lane & 31) + (lane >> 5)] = (uint16_t)(__float_as_uint(bf16r(o)) >> 16);
        lds_barrier();
        if (b < NRED) {
            const u64 nr = uni64(sm.ep[40]);
            const u32 rhi = (u32)(nr >> 32), rlo = (u32)nr;
            constexpr u32 YS = 1024;
            off = Y_OFF + b * 4096 + lo;
#pragma unroll
            for (int i = 0; i < 4; ++i) g[i] = ld_sc1(rws, off + i * YS);
#pragma nounroll
            for (u32 spin = 0; spin < SPIN_MAX; ++spin) {
                bool p[4];
#pragma unroll
                for (int i = 0; i < 4; ++i) p[i] = bad64(g[i], rhi, rlo) != 0;
                if (!__any(p[0] | p[1] | p[2] | p[3])) break;
                if (RSLEEP) __builtin_amdgcn_s_sleep(RSLEEP);
                asm volatile("" : "+v"(off));
#pragma unroll
                for (int i = 0; i < 4; ++i) if (p[i]) g[i] = ld_sc1(rws, off + i * YS);
            }
            const float t0 = (__uint_as_float(g[0][0]) + __uint_as_float(g[1][0])) + (__uint_as_float(g[2][0]) + __uint_as_float(g[3][0]));
            const float t1 = (__uint_as_float(g[0][1]) + __uint_as_float(g[1][1])) + (__uint_as_float(g[2][1]) + __uint_as_float(g[3][1]));
            float t = sw32_sum2(t0, t1);
            t = sw16_sum2(t, t); t += dpp<ROR8>(t); t += dpp<ROR4>(t);
            if ((lane & 28) == 0) out[b * 8 + 2 * (lane & 3) + (lane >> 5)] = (uint16_t)(__float_as_uint(bf16r(t)) >> 16);
            if (lane == 0) __hip_atomic_store((u64*)(ws + E_OFF) + b, nr, __ATOMIC_RELAXED, __HIP_MEMORY_SCOPE_AGENT);
        }
    } else if (wave < 11) {
        const int tp = wave < 5 ? wave : wave - 1;
#pragma nounroll
        for (u32 spin = 0; spin < SPIN_MAX * 16; ++spin) {
            if ((!WL || lds_ld(&sm.wdone) >= WN) && lds_ld(&sm.cnt[1]) >= NW) break;
            __builtin_amdgcn_s_sleep(WL ? 2 : GSL2);
        }
        asm volatile("" ::: "memory");
        u32x4 a[2][2];
        const uint16_t* wl = (const uint16_t*)&sm.wo[((tp >> 1) * 8 + r) * 64 + (tp & 1) * 4 + (c >> 3)] + c8;
#pragma unroll
        for (int t = 0; t < 2; ++t)
#pragma unroll
            for (int ks = 0; ks < 2; ++ks)
#pragma unroll
                for (int e = 0; e < 4; ++e) a[t][ks][e] = (u32)wl[(ks * 256 + 2 * e * 8 + t * 2) * 8] | ((u32)wl[(ks * 256 + (2 * e + 1) * 8 + t * 2) * 8] << 16);
        const int red = tp * 4 + (c & 2) + (r >> 1);
        const u64 ny = sm.ep[red < 40 ? red : 0];
        u32 yoff = Y_OFF + ((mat * 80 + ch * 40 + red) * 64 + h * 2 + rh) * 64 + ((r & 1) * 2 + (c & 1)) * 16;
        u32 yhi = (u32)(ny >> 32), ylo = (u32)ny;
        asm volatile("" : "+v"(a[0][0]), "+v"(a[0][1]), "+v"(a[1][0]), "+v"(a[1][1]), "+v"(yoff), "+v"(yhi), "+v"(ylo));
        __builtin_amdgcn_s_setprio(3);
        lds_barrier();
        const u32x4 b0 = sm.ov[r], b1 = sm.ov[4 + r];
        f32x4 d0 = {0.f, 0.f, 0.f, 0.f}, d1 = {0.f, 0.f, 0.f, 0.f};
        d0 = __builtin_amdgcn_mfma_f32_16x16x32_bf16(__builtin_bit_cast(bf16x8, a[0][0]), __builtin_bit_cast(bf16x8, b0), d0, 0, 0, 0);
        d1 = __builtin_amdgcn_mfma_f32_16x16x32_bf16(__builtin_bit_cast(bf16x8, a[1][0]), __builtin_bit_cast(bf16x8, b0), d1, 0, 0, 0);
        d0 = __builtin_amdgcn_mfma_f32_16x16x32_bf16(__builtin_bit_cast(bf16x8, a[0][1]), __builtin_bit_cast(bf16x8, b1), d0, 0, 0, 0);
        d1 = __builtin_amdgcn_mfma_f32_16x16x32_bf16(__builtin_bit_cast(bf16x8, a[1][1]), __builtin_bit_cast(bf16x8, b1), d1, 0, 0, 0);
        const f32x4 d = (c & 2) ? d1 : d0;
        const float y0 = (c & 1) ? d[2] : d[0], y1 = (c & 1) ? d[3] : d[1];
        if (c < 4) st_sc1(rws, yoff, u32x4{__float_as_uint(y0), __float_as_uint(y1), yhi, ylo});
    } else {
        lds_barrier();
        if (wave == 11 && j == 0 && lane == 0) __hip_atomic_store((u64*)(ws + G_OFF + h * 256), n1, __ATOMIC_RELAXED, __HIP_MEMORY_SCOPE_AGENT);
    }
}

extern "C" void kernel_launch(void* const* d_in, const int* in_sizes, int n_in,
                              void* d_out, int out_size, void* d_ws, size_t ws_size,
                              hipStream_t stream) {
    fused<<<NWG, 1024, 0, stream>>>((const uint16_t*)d_in[0], (const uint16_t*)d_in[1], (const uint16_t*)d_in[2], (const uint16_t*)d_in[3], (const uint16_t*)d_in[4],
                                    (const uint16_t*)d_in[5], (const uint16_t*)d_in[6], (const uint16_t*)d_in[7], (const uint16_t*)d_in[8], (const uint16_t*)d_in[9],
                                    (unsigned char*)d_ws, (uint16_t*)d_out);
}
```

```cpp
#include <hip/hip_runtime.h>
#include <hip/hip_bf16.h>
#include <stdint.h>

typedef unsigned int u32;
typedef unsigned long long u64;
typedef __attribute__((ext_vector_type(4))) unsigned int u32x4;
typedef __attribute__((ext_vector_type(2))) unsigned int u32x2;
typedef __attribute__((ext_vector_type(16))) unsigned int u32x16;
typedef __attribute__((ext_vector_type(4))) float f32x4;
typedef __attribute__((ext_vector_type(2))) float f32x2;
typedef __attribute__((ext_vector_type(2))) __bf16 bf16x2;
typedef __attribute__((ext_vector_type(8))) __bf16 bf16x8;
typedef __amdgpu_buffer_rsrc_t rsrc_t;

constexpr int NH = 32, SK = 8192, DH = 128, DM = 640, HD = NH * DH;
constexpr float SCALE = 0.08838834764831845f;
constexpr float LOG2E = 1.4426950408889634f;
constexpr int NWG = 256, NW = 16;
constexpr int NL = 40;
constexpr int NRED = 160;
#define PF 8
#define PFV 6
#define FA1 0
#define FA2 12
#define FA3 24
#define PW 2
#define NPROBE 2
#define GSL2 8
#define DSL 4
#define PCAP 200
#define GSLEEP 1
#define RSLEEP 1
#define WL 1
#define WN 4
#define WP0 6
#define TL 1
#define OS 16
#define EE 0
#define ES 1
#define PB 12
#define PG 1
#define XA 4
#define QSL 4
#define SSL 4
constexpr u32 SPOLL = 64;
constexpr u32 SPIN_MAX = 1u << 20;
constexpr u32 G_OFF = 2048;
constexpr u32 Q_OFF = 16384;
constexpr u32 R_OFF = Q_OFF + 32 * 1024;
constexpr u32 Y_OFF = R_OFF + 2 * 32 * 2 * 4096;
constexpr u32 D_OFF = Y_OFF + NRED * 64 * 64;
constexpr u32 S_OFF = D_OFF + 32 * 128;
constexpr u32 E_OFF = ES == 1 ? 0 : S_OFF + 32 * 8 * 16 * 256;
constexpr u32 WS_BYTES = S_OFF + 32 * 8 * 16 * 256 + NRED * 8 * ES;

__device__ __forceinline__ float dot2(u32 a, u32 b, float c) {
    return __builtin_amdgcn_fdot2_f32_bf16(__builtin_bit_cast(bf16x2, a), __builtin_bit_cast(bf16x2, b), c, false);
}
template <int CTRL> __device__ __forceinline__ float dpp(float x) {
    return __builtin_bit_cast(float, __builtin_amdgcn_mov_dpp(__builtin_bit_cast(int, x), CTRL, 0xf, 0xf, true));
}
constexpr int XOR1 = 0xB1, XOR2 = 0x4E, HMIR = 0x141, MIR = 0x140, ROR4 = 0x124, ROR8 = 0x128;
__device__ __forceinline__ float row16_sum(float x) {
    x += dpp<XOR1>(x); x += dpp<XOR2>(x); x += dpp<HMIR>(x); x += dpp<MIR>(x); return x;
}
__device__ __forceinline__ float row16_max(float x) {
    x = fmaxf(x, dpp<XOR1>(x)); x = fmaxf(x, dpp<XOR2>(x)); x = fmaxf(x, dpp<HMIR>(x)); x = fmaxf(x, dpp<MIR>(x)); return x;
}
__device__ __forceinline__ float sw16_sum2(float a, float b) {
    auto s = __builtin_amdgcn_permlane16_swap(__float_as_uint(a), __float_as_uint(b), false, false);
    return __uint_as_float(s[0]) + __uint_as_float(s[1]);
}
__device__ __forceinline__ float sw32_sum2(float a, float b) {
    auto t = __builtin_amdgcn_permlane32_swap(__float_as_uint(a), __float_as_uint(b), false, false);
    return __uint_as_float(t[0]) + __uint_as_float(t[1]);
}
__device__ __forceinline__ float xrow_sum(float x) { x = sw16_sum2(x, x); return sw32_sum2(x, x); }
__device__ __forceinline__ float sw32_max(float x) {
    auto t = __builtin_amdgcn_permlane32_swap(__float_as_uint(x), __float_as_uint(x), false, false);
    return fmaxf(__uint_as_float(t[0]), __uint_as_float(t[1]));
}
__device__ __forceinline__ float xrow_max(float x) {
    auto s = __builtin_amdgcn_permlane16_swap(__float_as_uint(x), __float_as_uint(x), false, false);
    return sw32_max(fmaxf(__uint_as_float(s[0]), __uint_as_float(s[1])));
}
__device__ __forceinline__ float rdlane(float x, int l) { return __builtin_bit_cast(float, __builtin_amdgcn_readlane(__builtin_bit_cast(int, x), l)); }
__device__ __forceinline__ float bf16r(float x) { return (float)(__bf16)x; }
__device__ __forceinline__ float lo16(u32 w) { return __uint_as_float(w << 16); }
__device__ __forceinline__ float hi16(u32 w) { return __uint_as_float(w & 0xffff0000u); }
__device__ __forceinline__ rsrc_t mkrsrc(const void* p, u32 bytes) { return __builtin_amdgcn_make_buffer_rsrc((void*)p, 0, bytes, 0x00020000); }
template <int AUX> __device__ __forceinline__ u32x4 bld(rsrc_t r, u32 off, u32 soff = 0) { return __builtin_amdgcn_raw_buffer_load_b128(r, off, soff, AUX); }
__device__ __forceinline__ u32x4 ld_sc1(rsrc_t r, u32 off, u32 soff = 0) { return __builtin_amdgcn_raw_buffer_load_b128(r, off, soff, 16); }
__device__ __forceinline__ void st_sc1(rsrc_t r, u32 off, u32x4 v) { __builtin_amdgcn_raw_buffer_store_b128(v, r, off, 0, 16); }
__device__ __forceinline__ void lds_barrier() { asm volatile("s_waitcnt lgkmcnt(0)\n\ts_barrier" ::: "memory"); }
__device__ __forceinline__ void lds_wait() { asm volatile("s_waitcnt lgkmcnt(0)" ::: "memory"); }
template <int N> __device__ __forceinline__ void vm_wait() { __builtin_amdgcn_s_waitcnt(0x0f70 | (N & 15) | ((N >> 4) << 14)); }
__device__ __forceinline__ u64 uni64(u64 v) { return ((u64)(u32)__builtin_amdgcn_readfirstlane((u32)(v >> 32)) << 32) | (u32)__builtin_amdgcn_readfirstlane((u32)v); }

__device__ __forceinline__ u64 sld64(const void* p) { u64 v; asm volatile("s_load_dwordx2 %0, %1, 0x0 glc\n\ts_waitcnt lgkmcnt(0)" : "=&s"(v) : "s"(p) : "memory"); return v; }
__device__ __forceinline__ u32x16 sld16(const void* p) { u32x16 v; asm volatile("s_load_dwordx16 %0, %1, 0x0 glc\n\ts_waitcnt lgkmcnt(0)" : "=&s"(v) : "s"(p) : "memory"); return v; }
__device__ __forceinline__ u32 tbad(const u32x16& d, u32 hi, u32 lo) {
    return ((d[2] ^ hi) | (d[3] ^ lo)) | ((d[6] ^ hi) | (d[7] ^ lo)) | ((d[10] ^ hi) | (d[11] ^ lo)) | ((d[14] ^ hi) | (d[15] ^ lo));
}
__device__ __forceinline__ u32 wrlane(u32 v, int l, u32 old) { asm("v_writelane_b32 %0, %1, %2" : "+v"(old) : "s"(v), "n"(l)); return old; }
__device__ __forceinline__ void glds16(const void* gsrc, u32 lds_dst) {
    u32 keep;
    asm volatile("s_mov_b32 %0, m0\n\ts_mov_b32 m0, %2\n\ts_nop 0\n\tglobal_load_lds_dwordx4 %1, off nt\n\ts_mov_b32 m0, %0" : "=&s"(keep) : "v"(gsrc), "s"(lds_dst) : "memory");
}
__device__ __forceinline__ u32 lds_ld(const u32* p) { return __builtin_amdgcn_readfirstlane(__hip_atomic_load(p, __ATOMIC_RELAXED, __HIP_MEMORY_SCOPE_WORKGROUP)); }
__device__ __forceinline__ u32 bad48(u32x4 g, u32 hi, u32 lo) { return (((g[0] ^ hi) | (g[1] ^ (hi >> 8))) & 255u) | (g[3] ^ lo); }
__device__ __forceinline__ u32 bad64(u32x4 g, u32 hi, u32 lo) { return (g[2] ^ hi) | (g[3] ^ lo); }

struct Smem {
    u32x4 wo[40 * 64];
    float part[2][NW][128];
    float ml[2][NW][2];
    u32x4 ov[8];
    float qp[4][32];
    u64 ep[48], n1;
    u32 qv[128];
    u32 qcnt, q4, cnt[2], dec, pdone, wdone;
};

__device__ __forceinline__ void combine_publish(Smem& sm, int ab, int lane, rsrc_t rws, int h, int slot, u32 nhi, u32 nlo) {
    const f32x2 ml = *(const f32x2*)&sm.ml[ab][lane & 15][0];
    f32x2 pv[NW];
#pragma unroll
    for (int w = 0; w < NW; ++w) pv[w] = *(const f32x2*)&sm.part[ab][w][2 * lane];
    const u32 off = R_OFF + (((ab * 32 + h) * 2 + (lane >> 5)) * 8 + slot) * 512 + (lane & 31) * 16;
    __builtin_amdgcn_sched_barrier(0);
    const float M = row16_max(ml[0]);
    const float f = __builtin_amdgcn_exp2f(ml[0] - M);
    const float L = row16_sum(f * ml[1]);
    f32x2 o[4];
#pragma unroll
    for (int w = 0; w < NW; ++w) { if (w < 4) o[w] = rdlane(f, w) * pv[w]; else o[w & 3] += rdlane(f, w) * pv[w]; }
    const f32x2 t = ((o[0] + o[1]) + (o[2] + o[3])) * __builtin_amdgcn_rcpf(L);
    const float W = M + __builtin_amdgcn_logf(L);
    st_sc1(rws, off, u32x4{(__float_as_uint(t[0]) & ~255u) | (nhi & 255u), (__float_as_uint(t[1]) & ~255u) | ((nhi >> 8) & 255u), __float_as_uint(W), nlo});
}

__global__ void __launch_bounds__(1024) fused(const uint16_t* __restrict__ xA, const uint16_t* __restrict__ kA, const uint16_t* __restrict__ vA,
                                              const uint16_t* __restrict__ wqA, const uint16_t* __restrict__ woA,
                                              const uint16_t* __restrict__ xB, const uint16_t* __restrict__ kB, const uint16_t* __restrict__ vB,
                                              const uint16_t* __restrict__ wqB, const uint16_t* __restrict__ woB,
                                              unsigned char* __restrict__ ws, uint16_t* __restrict__ out) {
    __shared__ __attribute__((aligned(16))) Smem sm;
    const int b = blockIdx.x;
    const int h = b >> 3, j = b & 7;
    const int tid = threadIdx.x, lane = tid & 63, wave = __builtin_amdgcn_readfirstlane(tid >> 6), r = lane >> 4, c = lane & 15;
    const int row8 = lane >> 3, c8 = lane & 7;
    const int mat = j >> 2, rh = (j >> 1) & 1, ch = j & 1;
    const rsrc_t rws = mkrsrc(ws, WS_BYTES);
    const rsrc_t rkA = mkrsrc(kA, NH * SK * DH * 2), rvA = mkrsrc(vA, NH * SK * DH * 2);
    const rsrc_t rkB = mkrsrc(kB, NH * SK * DH * 2), rvB = mkrsrc(vB, NH * SK * DH * 2);
    const u32 lo = lane * 16;
#define CLS(k) ((u32)((((k) & 1) << 14) | (((k) & 2) << 11) | (((k) & 4) << 11)))
    const u32 soB = (u32)h * (SK * 256) + ((48 + wave) << 15) + CLS(j ^ (wave & 7));
    u32x4 buf[NL];
#define GX(i) ((u32)(((i) << 19) | (((i) & 1) << 14) | (((i) >> 1) << 12)))
#define LD(n) ((n) < 8 ? bld<2>(((n) & 4) ? rvB : rkB, lo + ((n) & 3) * 1024, soB) : (n) < 24 ? bld<2>(rkA, lo + ((n) & 3) * 1024, soK ^ GX(((n) - 8) >> 2)) : bld<2>(rvA, lo + ((n) & 3) * 1024, soV ^ GX(((n) - 24) >> 2)))
    __builtin_amdgcn_s_setprio(3);
    u32 soK = 0, soV = 0;
    if (wave < 4) {
        const int hq = (h & ~1) | (j >> 2), qm = (j >> 1) & 1, qd0 = (j & 1) * 64 + (h & 1) * 32;
        const rsrc_t rw = mkrsrc(qm ? wqB : wqA, DM * HD * 2), rx = mkrsrc(qm ? xB : xA, DM * 2);
        const u32 wb = (wave * 160 * HD + hq * DH + qd0) * 2;
        const u32x4 x4 = bld<0>(rx, 320 * wave + 20 * (lane >> 2));
        const u32 x1 = __builtin_amdgcn_raw_buffer_load_b32(rx, 320 * wave + 20 * (lane >> 2) + 16, 0, 0);
        u32x4 w[10];
#pragma unroll
        for (int i = 0; i < 10; ++i) w[i] = bld<2>(rw, ((lane >> 2) * 10 * HD + (lane & 3) * 8) * 2, wb + i * HD * 2);
        __builtin_amdgcn_sched_barrier(0);
        lds_barrier();
        const u64 nq = sld64(ws + G_OFF + hq * 256) + 1;
        __builtin_amdgcn_sched_barrier(0);
        float q[8];
#pragma unroll
        for (int k = 0; k < 8; ++k) q[k] = 0.f;
#pragma unroll
        for (int i = 0; i < 10; ++i) {
            const u32 xw = i < 8 ? x4[i >> 1] : x1;
            const float xs = (i & 1) ? hi16(xw) : lo16(xw);
#pragma unroll
            for (int k = 0; k < 4; ++k) { q[2 * k] = fmaf(xs, lo16(w[i][k]), q[2 * k]); q[2 * k + 1] = fmaf(xs, hi16(w[i][k]), q[2 * k + 1]); }
        }
#pragma unroll
        for (int k = 0; k < 8; ++k) { q[k] += dpp<ROR4>(q[k]); q[k] += dpp<ROR8>(q[k]); q[k] = xrow_sum(q[k]); }
        if (lane < 4) { *(f32x4*)&sm.qp[wave][8 * lane] = f32x4{q[0], q[1], q[2], q[3]}; *(f32x4*)&sm.qp[wave][8 * lane + 4] = f32x4{q[4], q[5], q[6], q[7]}; }
        u32 old = 0;
        lds_wait();
        if (lane == 0) old = __hip_atomic_fetch_add(&sm.q4, 1u, __ATOMIC_RELAXED, __HIP_MEMORY_SCOPE_WORKGROUP);
        if (__builtin_amdgcn_readfirstlane(old) == 3) {
            asm volatile("" ::: "memory");
            const f32x4 s = (*(const f32x4*)&sm.qp[0][4 * c8] + *(const f32x4*)&sm.qp[1][4 * c8]) + (*(const f32x4*)&sm.qp[2][4 * c8] + *(const f32x4*)&sm.qp[3][4 * c8]);
            const u32 p0 = (__float_as_uint(bf16r(s[0])) >> 16) | (__float_as_uint(bf16r(s[1])) & 0xffff0000u);
            const u32 p1 = (__float_as_uint(bf16r(s[2])) >> 16) | (__float_as_uint(bf16r(s[3])) & 0xffff0000u);
            if (lane < 8) st_sc1(rws, Q_OFF + hq * 1024 + (qm * 128 + qd0) * 4 + lo, u32x4{p0, p1, (u32)(nq >> 32), (u32)nq});
        }
#pragma nounroll
        for (u32 spin = 0; spin < SPIN_MAX * 16; ++spin) {
            if (lds_ld(&sm.pdone)) break;
            __builtin_amdgcn_s_sleep(2);
        }
        asm volatile("" ::: "memory");
        {
            const u64 n1 = uni64(sm.n1);
            const u32 nhi = (u32)(n1 >> 32), nlo = (u32)n1;
            const u32 qb = __builtin_amdgcn_readfirstlane(Q_OFF + h * 1024 + wave * 256);
            bool ok = false;
            u32x16 d0, d1, d2, d3;
#pragma nounroll
            for (u32 spin = 0; spin < SPOLL; ++spin) {
                asm volatile("s_load_dwordx16 %0, %4, 0x0 glc\n\ts_load_dwordx16 %1, %4, 0x40 glc\n\ts_load_dwordx16 %2, %4, 0x80 glc\n\ts_load_dwordx16 %3, %4, 0xc0 glc\n\ts_waitcnt lgkmcnt(0)"
                             : "=&s"(d0), "=&s"(d1), "=&s"(d2), "=&s"(d3) : "s"(ws + qb) : "memory");
                if (((tbad(d0, nhi, nlo) | tbad(d1, nhi, nlo)) | (tbad(d2, nhi, nlo) | tbad(d3, nhi, nlo))) == 0) { ok = true; break; }
                __builtin_amdgcn_s_sleep(QSL);
            }
            u32 g0 = 0, g1 = 0;
            if (ok) {
#pragma unroll
                for (int i = 0; i < 4; ++i) {
                    g0 = wrlane(d0[4 * i], i, g0); g1 = wrlane(d0[4 * i + 1], i, g1);
                    g0 = wrlane(d1[4 * i], 4 + i, g0); g1 = wrlane(d1[4 * i + 1], 4 + i, g1);
                    g0 = wrlane(d2[4 * i], 8 + i, g0); g1 = wrlane(d2[4 * i + 1], 8 + i, g1);
                    g0 = wrlane(d3[4 * i], 12 + i, g0); g1 = wrlane(d3[4 * i + 1], 12 + i, g1);
                }
            } else {
                u32 off = qb + c * 16;
                u32x4 g;
#pragma nounroll
                for (u32 spin = 0; spin < SPIN_MAX; ++spin) {
                    asm volatile("" : "+v"(off));
                    g = ld_sc1(rws, off);
                    if (__all(bad64(g, nhi, nlo) == 0)) break;
                    __builtin_amdgcn_s_sleep(1);
                }
                g0 = g[0]; g1 = g[1];
            }
            if (lane < 16) *(u32x2*)&sm.qv[wave * 32 + 2 * lane] = u32x2{g0, g1};
            lds_wait();
            if (lane == 0) __hip_atomic_fetch_add(&sm.qcnt, 1u, __ATOMIC_RELAXED, __HIP_MEMORY_SCOPE_WORKGROUP);
        }
        __builtin_amdgcn_sched_barrier(0);
#pragma unroll
        for (int n = 0; n < 8; ++n) {
            if (n >= PW && lds_ld(&sm.dec) == 0) vm_wait<PW - 1>();
            buf[n] = LD(n);
            __builtin_amdgcn_sched_barrier(0);
        }
    } else if (wave == 12) {
        if (lane == 0) { sm.qcnt = 0; sm.q4 = 0; sm.cnt[0] = 0; sm.cnt[1] = 0; sm.dec = 0; sm.pdone = 0; sm.wdone = 0; }
        lds_barrier();
        int gs = 0;
        {
            const unsigned char* pa = (const unsigned char*)((j & 1) ? vA : kA) + (size_t)h * (SK * 256) + (j >> 1) * (16 * 32768);
#pragma unroll
            for (int i = 0; i < NPROBE; ++i) {
                const int side = ((j >> 1) ^ i) & 1;
                const unsigned char* p1 = pa + i * 32768 + side * 8192;
                const unsigned char* p2 = pa + i * 32768 + (side ^ 1) * 8192;
                u32 v1, v2; u64 t1, t2;
                asm volatile("s_load_dword %0, %4, 0x0 glc\n\ts_load_dword %1, %5, 0x0 glc\n\ts_waitcnt lgkmcnt(1)\n\ts_memtime %2\n\ts_waitcnt lgkmcnt(0)\n\ts_memtime %3\n\ts_waitcnt lgkmcnt(0)"
                             : "=&s"(v1), "=&s"(v2), "=&s"(t1), "=&s"(t2) : "s"(p1), "s"(p2) : "memory");
                const int g = min((int)(u32)(t2 - t1), PCAP);
                gs += side ? -g : g;
            }
        }
        u64 e1;
        {
            u32 dummy;
            asm volatile("s_load_dword %1, %3, 0x0\n\ts_load_dwordx2 %0, %2, 0x0 glc\n\ts_waitcnt lgkmcnt(0)" : "=&s"(e1), "=&s"(dummy)
                         : "s"(ws + G_OFF + h * 256), "s"((const unsigned char*)((j & 1) ? kA : vA) + (size_t)h * (SK * 256) + j * 4096) : "memory");
        }
        const u64 n1 = e1 + 1;
        const u32 nhi = (u32)(n1 >> 32), nlo = (u32)n1;
        const int iod0 = (__builtin_amdgcn_s_getreg((20 << 0) | (0 << 6) | (3 << 11)) >> 2) & 1;
        if (lane == 0) st_sc1(rws, D_OFF + h * 128 + j * 16, u32x4{(u32)(iod0 ? -gs : gs), (u32)iod0, nhi, nlo});
        if (lane == 0) { sm.n1 = n1; lds_wait(); __hip_atomic_store(&sm.pdone, 1u, __ATOMIC_RELAXED, __HIP_MEMORY_SCOPE_WORKGROUP); }
        int vg[8], vi[8];
        bool ok = false;
#pragma nounroll
        for (u32 spin = 0; spin < SPOLL; ++spin) {
            __builtin_amdgcn_s_sleep(2);
            u32x16 d0, d1;
            asm volatile("s_load_dwordx16 %0, %2, 0x0 glc\n\ts_load_dwordx16 %1, %2, 0x40 glc\n\ts_waitcnt lgkmcnt(0)" : "=&s"(d0), "=&s"(d1) : "s"(ws + (u32)__builtin_amdgcn_readfirstlane(D_OFF + h * 128)) : "memory");
#pragma unroll
            for (int i = 0; i < 4; ++i) { vg[i] = d0[4 * i]; vi[i] = d0[4 * i + 1]; vg[4 + i] = d1[4 * i]; vi[4 + i] = d1[4 * i + 1]; }
            if ((tbad(d0, nhi, nlo) | tbad(d1, nhi, nlo)) == 0) { ok = true; break; }
        }
        if (!ok) {
            u32 off = D_OFF + h * 128 + c8 * 16;
            u32x4 g;
#pragma nounroll
            for (u32 spin = 0; spin < SPIN_MAX; ++spin) {
                asm volatile("" : "+v"(off));
                g = ld_sc1(rws, off);
                if (__all(bad64(g, nhi, nlo) == 0)) break;
                __builtin_amdgcn_s_sleep(1);
            }
#pragma unroll
            for (int i = 0; i < 8; ++i) { vg[i] = __builtin_amdgcn_readlane(g[0], i); vi[i] = __builtin_amdgcn_readlane(g[1], i); }
        }
        int msk = 0;
#pragma unroll
        for (int i = 0; i < 8; ++i) msk |= (vi[i] & 1) << i;
        const int sk = (vg[0] + vg[2]) + (vg[4] + vg[6]), sv = (vg[1] + vg[3]) + (vg[5] + vg[7]);
        const int iod = (msk >> j) & 1;
        const int rank = __builtin_popcount((iod ? msk : ~msk) & ((1 << j) - 1));
        const int pk_ = sk > 0 ? 0 : 1, pv_ = sv > 0 ? 0 : 1;
        const bool bal = __builtin_popcount(msk) == 4;
        const u32 d = 0x100u | (bal ? ((iod ^ pk_) * 4 + rank) | (((iod ^ pv_) * 4 + rank) << 4) : j * 17);
        if (lane == 0) __hip_atomic_store(&sm.dec, d, __ATOMIC_RELAXED, __HIP_MEMORY_SCOPE_WORKGROUP);
        __builtin_amdgcn_sched_barrier(0);
#pragma unroll
        for (int n = 0; n < 8; ++n) { buf[n] = LD(n); __builtin_amdgcn_sched_barrier(0); }
    } else {
        lds_barrier();
        if (!WL && wave < 12) {
            const uint16_t* wsrc = (mat ? woB : woA) + (size_t)(h * DH + rh * 64 + row8) * DM + ch * 320 + c8 * 8;
#pragma unroll
            for (int i = 0; i < 5; ++i) {
                const int idx = (wave - 4) * 5 + i;
                if (i >= PW) vm_wait<PW - 1>();
                glds16(wsrc + (idx & 7) * 8 * DM + (idx >> 3) * 64, (u32)(uintptr_t)&sm.wo[idx * 64]);
            }
        }
        __builtin_amdgcn_sched_barrier(0);
        const u64 ev = __hip_atomic_load((const u64*)(ws + E_OFF) + (lane < 40 ? mat * 80 + ch * 40 + lane : b < NRED ? b : 0) * ES, __ATOMIC_RELAXED, __HIP_MEMORY_SCOPE_AGENT);
#pragma unroll
        for (int n = 0; n < 8; ++n) {
            if (lds_ld(&sm.dec) == 0) vm_wait<PW - 1>();
            buf[n] = LD(n);
            __builtin_amdgcn_sched_barrier(0);
        }
        if (wave == 8 && lane < 48) sm.ep[lane] = ev + 1;
    }
    __builtin_amdgcn_sched_barrier(0);
    u32 dec = 0;
#pragma nounroll
    for (u32 spin = 0; spin < SPIN_MAX * 16; ++spin) {
        dec = lds_ld(&sm.dec);
        if (dec) break;
        __builtin_amdgcn_s_sleep(DSL);
    }
    const int kK = dec & 7, kV = (dec >> 4) & 7;
    soK = (u32)h * (SK * 256) + (wave << 15) + CLS(kK);
    soV = (u32)h * (SK * 256) + (wave << 15) + CLS(kV);
    __builtin_amdgcn_sched_barrier(0);
#pragma unroll
    for (int n = 8; n < 8 + PF; ++n) { buf[n] = LD(n); __builtin_amdgcn_sched_barrier(0); }
    __builtin_amdgcn_sched_barrier(0);
#pragma nounroll
    for (u32 spin = 0; spin < SPIN_MAX * 16; ++spin) {
        if (lds_ld(&sm.qcnt) == 4) break;
        __builtin_amdgcn_s_sleep(DSL);
    }
    asm volatile("" ::: "memory");
    const u32x4 qa4 = *(const u32x4*)&sm.qv[4 * c], qb4 = *(const u32x4*)&sm.qv[64 + 4 * c];
    const u64 n1 = uni64(sm.n1);
    const u32 nhi = (u32)(n1 >> 32), nlo = (u32)n1;
    float acc[8], m = -INFINITY, l = 0.f, sc[4];
#pragma unroll
    for (int k = 0; k < 8; ++k) acc[k] = 0.f;
#define FLUSH(AB) do { const float t0_ = sw32_sum2(acc[0], acc[4]), t1_ = sw32_sum2(acc[1], acc[5]), t2_ = sw32_sum2(acc[2], acc[6]), t3_ = sw32_sum2(acc[3], acc[7]); \
        *(f32x2*)&sm.part[AB][wave][8 * c + 2 * r] = f32x2{sw16_sum2(t0_, t2_), sw16_sum2(t1_, t3_)}; } while (0)
#define PUTML(AB) do { l = xrow_sum(l); if (lane == 0) *(f32x2*)&sm.ml[AB][wave][0] = f32x2{m, l}; } while (0)
#define TS(n) ((n) < 8 ? (n) + 16 : (n) < 24 ? (n) - 8 : (n))
#define NEXT(n) do { __builtin_amdgcn_sched_barrier(0); \
        if (TS(n) == FA1) __builtin_amdgcn_s_setprio(2); if (TS(n) == FA2) __builtin_amdgcn_s_setprio(1); if (TS(n) == FA3) __builtin_amdgcn_s_setprio(0); \
        if ((n) >= 8 && (n) + PF < 24) buf[(n) + PF] = LD((n) + PF); \
        if (TS(n) + PFV >= 24 && TS(n) + PFV < NL) buf[TS(n) + PFV] = LD(TS(n) + PFV); \
        __builtin_amdgcn_sched_barrier(0); } while (0)
#define SCORE(B, Q) ({ float d_ = dot2((B)[0], (Q)[0], 0.f); d_ = dot2((B)[1], (Q)[1], d_); d_ = dot2((B)[2], (Q)[2], d_); d_ = dot2((B)[3], (Q)[3], d_); \
                       d_ = row16_sum(d_); bf16r(bf16r(d_) * SCALE); })
#define PV(B, P) do { _Pragma("unroll") for (int k = 0; k < 4; ++k) { acc[2 * k] = fmaf((P), lo16((B)[k]), acc[2 * k]); acc[2 * k + 1] = fmaf((P), hi16((B)[k]), acc[2 * k + 1]); } \
        asm volatile("" : "+v"(acc[0]), "+v"(acc[1]), "+v"(acc[2]), "+v"(acc[3]), "+v"(acc[4]), "+v"(acc[5]), "+v"(acc[6]), "+v"(acc[7]) :: "memory"); } while (0)
    const bool xch = kK != kV;
    u32 pk[8];
#pragma unroll
    for (int n = 8; n < 24; ++n) {
        const u32 sb = __float_as_uint(SCORE(buf[n], qa4));
        if (n & 1) pk[(n - 8) >> 1] |= sb & 0xffff0000u; else pk[(n - 8) >> 1] = sb >> 16;
        asm volatile("" : "+v"(pk[(n - 8) >> 1]) :: "memory");
        if (n == 23 && xch) {
            u32 w0 = pk[0], w1 = pk[1];
#pragma unroll
            for (int k = 1; k < 4; ++k) { w0 = c == k ? pk[2 * k] : w0; w1 = c == k ? pk[2 * k + 1] : w1; }
            if (c < 4) st_sc1(rws, S_OFF + ((h * 8 + kK) * 16 + wave) * 256 + c * 64 + r * 16, u32x4{w0, w1, nhi, nlo});
        }
        NEXT(n);
    }
#pragma unroll
    for (int n = 0; n < 4; ++n) { sc[n] = SCORE(buf[n], qb4) * LOG2E; asm volatile("" : "+v"(sc[n]) :: "memory"); NEXT(n); }
    m = xrow_max(fmaxf(fmaxf(sc[0], sc[1]), fmaxf(sc[2], sc[3])));
#pragma unroll
    for (int n = 4; n < 8; ++n) { const float p = __builtin_amdgcn_exp2f(sc[n & 3] - m); l += p; PV(buf[n], p); NEXT(n); }
    {
        PUTML(1);
        FLUSH(1);
        u32 old = 0;
        lds_wait();
        if (lane == 0) old = __hip_atomic_fetch_add(&sm.cnt[1], 1u, __ATOMIC_RELAXED, __HIP_MEMORY_SCOPE_WORKGROUP);
        if (PB < 0 && __builtin_amdgcn_readfirstlane(old) == NW - 1) { asm volatile("" ::: "memory"); combine_publish(sm, 1, lane, rws, h, j, nhi, nlo); }
        l = 0.f;
#pragma unroll
        for (int k = 0; k < 8; ++k) acc[k] = 0.f;
        __builtin_amdgcn_sched_barrier(0);
    }
    const u32 sb = __builtin_amdgcn_readfirstlane(S_OFF + ((h * 8 + kV) * 16 + wave) * 256);
#define VPHASE(I0) do { \
        m = xrow_max(fmaxf(fmaxf(fmaxf(fmaxf(lo16(pk[0]), hi16(pk[0])), fmaxf(lo16(pk[1]), hi16(pk[1]))), fmaxf(fmaxf(lo16(pk[2]), hi16(pk[2])), fmaxf(lo16(pk[3]), hi16(pk[3])))), \
                           fmaxf(fmaxf(fmaxf(lo16(pk[4]), hi16(pk[4])), fmaxf(lo16(pk[5]), hi16(pk[5]))), fmaxf(fmaxf(lo16(pk[6]), hi16(pk[6])), fmaxf(lo16(pk[7]), hi16(pk[7])))))) * LOG2E; \
        _Pragma("unroll") for (int i = 0; i < 4; ++i) { \
            const u32 s0 = pk[2 * i], s1 = pk[2 * i + 1]; \
            sc[0] = __builtin_amdgcn_exp2f(lo16(s0) * LOG2E - m); sc[1] = __builtin_amdgcn_exp2f(hi16(s0) * LOG2E - m); \
            sc[2] = __builtin_amdgcn_exp2f(lo16(s1) * LOG2E - m); sc[3] = __builtin_amdgcn_exp2f(hi16(s1) * LOG2E - m); \
            l += (sc[0] + sc[1]) + (sc[2] + sc[3]); \
            if (i == 3) PUTML(0); \
            __builtin_amdgcn_sched_barrier(0); \
            _Pragma("unroll") for (int n = 24 + 4 * i; n < 28 + 4 * i; ++n) { PV(buf[n], sc[n & 3]); __builtin_amdgcn_sched_barrier(0); \
                if (n == FA3) __builtin_amdgcn_s_setprio(0); \
                if (n + PFV >= (I0) && n + PFV < NL) buf[n + PFV] = LD(n + PFV); \
                __builtin_amdgcn_sched_barrier(0); } \
        } \
        FLUSH(0); } while (0)
#define SPREAD(D) do { _Pragma("unroll") for (int i = 0; i < 4; ++i) { \
            _Pragma("unroll") for (int k = 0; k < 4; ++k) { pk[2 * i] = wrlane((D)[i][4 * k], 16 * k, pk[2 * i]); pk[2 * i + 1] = wrlane((D)[i][4 * k + 1], 16 * k, pk[2 * i + 1]); } } \
        asm volatile("s_nop 1" : "+v"(pk[6]), "+v"(pk[7])); \
        _Pragma("unroll") for (int k = 0; k < 8; ++k) pk[k] = __builtin_amdgcn_mov_dpp(pk[k], 0x150, 0xf, 0xf, true); } while (0)
#define SLD4(D) asm volatile("s_load_dwordx16 %0, %4, 0x0 glc\n\ts_load_dwordx16 %1, %4, 0x40 glc\n\ts_load_dwordx16 %2, %4, 0x80 glc\n\ts_load_dwordx16 %3, %4, 0xc0 glc\n\ts_waitcnt lgkmcnt(0)" \
                     : "=&s"((D)[0]), "=&s"((D)[1]), "=&s"((D)[2]), "=&s"((D)[3]) : "s"(ws + sb) : "memory")
#define TB4(D) ((tbad((D)[0], nhi, nlo) | tbad((D)[1], nhi, nlo)) | (tbad((D)[2], nhi, nlo) | tbad((D)[3], nhi, nlo)))
    bool late = false;
    if (xch) {
        u32x16 d[4];
        SLD4(d);
        late = TB4(d) != 0;
        if (!late) SPREAD(d);
    }
    if (late) {
#pragma unroll
        for (int n = 24 + PFV; n < 24 + PFV + XA; ++n) { buf[n] = LD(n); __builtin_amdgcn_sched_barrier(0); }
        bool ok = false;
        u32x16 d[4];
#pragma nounroll
        for (u32 spin = 0; spin < SPOLL; ++spin) {
            SLD4(d);
            if (TB4(d) == 0) { ok = true; break; }
            __builtin_amdgcn_s_sleep(SSL);
        }
        if (ok) SPREAD(d);
        else {
            u32 soff = sb + (c & 3) * 64 + r * 16;
            u32x4 g;
#pragma nounroll
            for (u32 spin = 0; spin < SPIN_MAX; ++spin) {
                asm volatile("" : "+v"(soff));
                g = ld_sc1(rws, soff);
                if (__all(bad64(g, nhi, nlo) == 0)) break;
                __builtin_amdgcn_s_sleep(1);
            }
            pk[0] = __builtin_amdgcn_mov_dpp(g[0], 0x150, 0xf, 0xf, true); pk[1] = __builtin_amdgcn_mov_dpp(g[1], 0x150, 0xf, 0xf, true);
            pk[2] = __builtin_amdgcn_mov_dpp(g[0], 0x151, 0xf, 0xf, true); pk[3] = __builtin_amdgcn_mov_dpp(g[1], 0x151, 0xf, 0xf, true);
            pk[4] = __builtin_amdgcn_mov_dpp(g[0], 0x152, 0xf, 0xf, true); pk[5] = __builtin_amdgcn_mov_dpp(g[1], 0x152, 0xf, 0xf, true);
            pk[6] = __builtin_amdgcn_mov_dpp(g[0], 0x153, 0xf, 0xf, true); pk[7] = __builtin_amdgcn_mov_dpp(g[1], 0x153, 0xf, 0xf, true);
        }
        if (XA) VPHASE(24 + PFV + XA);
    }
    if (!late || !XA) VPHASE(24 + PFV);
    {
        u32 old = 0;
        lds_wait();
        if (lane == 0) old = __hip_atomic_fetch_add(&sm.cnt[0], wave == 5 ? 1u : 0x101u, __ATOMIC_RELAXED, __HIP_MEMORY_SCOPE_WORKGROUP);
        old = __builtin_amdgcn_readfirstlane(old);
        if ((old & 255) == NW - 1) {
            asm volatile("" ::: "memory"); __builtin_amdgcn_s_setprio(3); combine_publish(sm, 0, lane, rws, h, kV, nhi, nlo);
        }
        if (PB >= 0 && (old & 255) == PB) {
#pragma nounroll
            for (u32 spin = 0; spin < SPIN_MAX * 16; ++spin) {
                if (lds_ld(&sm.cnt[1]) >= NW) break;
                __builtin_amdgcn_s_sleep(GSL2);
            }
            asm volatile("" ::: "memory");
            combine_publish(sm, 1, lane, rws, h, j, nhi, nlo);
        }
        const u32 pos = (old >> 8) - WP0;
        if (WL && wave != 5 && pos < WN) {
            const uint16_t* wsrc = (mat ? woB : woA) + (size_t)(h * DH + rh * 64 + row8) * DM + ch * 320 + c8 * 8;
#pragma unroll
            for (int i = 0; i < 40 / WN; ++i) {
                const int idx = pos * (40 / WN) + i;
                glds16(wsrc + (idx & 7) * 8 * DM + (idx >> 3) * 64, (u32)(uintptr_t)&sm.wo[idx * 64]);
            }
            vm_wait<0>();
            if (lane == 0) __hip_atomic_fetch_add(&sm.wdone, 1u, __ATOMIC_RELAXED, __HIP_MEMORY_SCOPE_WORKGROUP);
        }
    }
    if (wave == 5) {
#pragma nounroll
        for (u32 spin = 0; spin < SPIN_MAX * 16; ++spin) {
            if (PB >= 0 && PG ? (lds_ld(&sm.cnt[0]) & 255) >= (mat ? PB + 1 : NW) : (lds_ld(&sm.cnt[mat]) & 255) >= NW) break;
            __builtin_amdgcn_s_sleep(GSL2);
        }
        __builtin_amdgcn_s_setprio(3);
        u32 off = R_OFF + ((mat * 32 + h) * 2 + rh) * 4096 + lo;
        u32x4 g[4];
#pragma unroll
        for (int i = 0; i < 4; ++i) g[i] = ld_sc1(rws, off + i * 1024);
#pragma nounroll
        for (u32 spin = 0; spin < SPIN_MAX; ++spin) {
            bool p[4];
#pragma unroll
            for (int i = 0; i < 4; ++i) p[i] = bad48(g[i], nhi, nlo) != 0;
            if (!__any(p[0] | p[1] | p[2] | p[3])) break;
            if (GSLEEP) __builtin_amdgcn_s_sleep(GSLEEP);
            asm volatile("" : "+v"(off));
#pragma unroll
            for (int i = 0; i < 4; ++i) if (p[i]) g[i] = ld_sc1(rws, off + i * 1024);
        }
        const float W = sw32_max(fmaxf(fmaxf(__uint_as_float(g[0][2]), __uint_as_float(g[1][2])), fmaxf(__uint_as_float(g[2][2]), __uint_as_float(g[3][2]))));
        float o0 = 0.f, o1 = 0.f, S = 0.f;
#pragma unroll
        for (int i = 0; i < 4; ++i) {
            const float e = __builtin_amdgcn_exp2f(__uint_as_float(g[i][2]) - W);
            S += e; o0 = fmaf(e, __uint_as_float(g[i][0] & ~255u), o0); o1 = fmaf(e, __uint_as_float(g[i][1] & ~255u), o1);
        }
        const float o = sw32_sum2(o0, o1) * __builtin_amdgcn_rcpf(sw32_sum2(S, S));
        ((uint16_t*)sm.ov)[2 * (lane & 31) + (lane >> 5)] = (uint16_t)(__float_as_uint(bf16r(o)) >> 16);
        lds_barrier();
        if (b < NRED) {
            const u64 nr = uni64(sm.ep[40]);
            const u32 rhi = (u32)(nr >> 32), rlo = (u32)nr;
            constexpr u32 YS = 1024;
            off = Y_OFF + b * 4096 + lo;
#pragma unroll
            for (int i = 0; i < 4; ++i) g[i] = ld_sc1(rws, off + i * YS);
#pragma nounroll
            for (u32 spin = 0; spin < SPIN_MAX; ++spin) {
                bool p[4];
#pragma unroll
                for (int i = 0; i < 4; ++i) p[i] = bad64(g[i], rhi, rlo) != 0;
                if (!__any(p[0] | p[1] | p[2] | p[3])) break;
                if (RSLEEP) __builtin_amdgcn_s_sleep(RSLEEP);
                asm volatile("" : "+v"(off));
#pragma unroll
                for (int i = 0; i < 4; ++i) if (p[i]) g[i] = ld_sc1(rws, off + i * YS);
            }
            if (EE && lane == 0) __hip_atomic_store((u64*)(ws + E_OFF) + b * ES, nr, __ATOMIC_RELAXED, __HIP_MEMORY_SCOPE_AGENT);
            __builtin_amdgcn_sched_barrier(0);
            const float t0 = (__uint_as_float(g[0][0]) + __uint_as_float(g[1][0])) + (__uint_as_float(g[2][0]) + __uint_as_float(g[3][0]));
            const float t1 = (__uint_as_float(g[0][1]) + __uint_as_float(g[1][1])) + (__uint_as_float(g[2][1]) + __uint_as_float(g[3][1]));
            float t = sw32_sum2(t0, t1);
            t = sw16_sum2(t, t); t += dpp<ROR8>(t); t += dpp<ROR4>(t);
            if ((lane & 28) == 0) __builtin_amdgcn_raw_buffer_store_b16((uint16_t)(__float_as_uint(bf16r(t)) >> 16), mkrsrc(out, 2 * 2 * DM), (b * 8 + 2 * (lane & 3) + (lane >> 5)) * 2, 0, OS);
            if (!EE && lane == 0) __hip_atomic_store((u64*)(ws + E_OFF) + b * ES, nr, __ATOMIC_RELAXED, __HIP_MEMORY_SCOPE_AGENT);
        }
    } else if (wave < 11) {
        const int tp = wave < 5 ? wave : wave - 1;
#pragma nounroll
        for (u32 spin = 0; spin < SPIN_MAX * 16; ++spin) {
            if ((!WL || lds_ld(&sm.wdone) >= WN) && lds_ld(&sm.cnt[1]) >= NW) break;
            __builtin_amdgcn_s_sleep(WL ? 2 : GSL2);
        }
        asm volatile("" ::: "memory");
        u32x4 a[2][2];
        const uint16_t* wl = (const uint16_t*)&sm.wo[((tp >> 1) * 8 + r) * 64 + (tp & 1) * 4 + (c >> 3)] + c8;
#pragma unroll
        for (int t = 0; t < 2; ++t)
#pragma unroll
            for (int ks = 0; ks < 2; ++ks)
#pragma unroll
                for (int e = 0; e < 4; ++e) a[t][ks][e] = (u32)wl[(ks * 256 + 2 * e * 8 + t * 2) * 8] | ((u32)wl[(ks * 256 + (2 * e + 1) * 8 + t * 2) * 8] << 16);
        const int red = tp * 4 + (c & 2) + (r >> 1);
        const u64 ny = sm.ep[red < 40 ? red : 0];
        u32 yoff = Y_OFF + ((mat * 80 + ch * 40 + red) * 64 + h * 2 + rh) * 64 + ((r & 1) * 2 + (c & 1)) * 16;
        u32 yhi = (u32)(ny >> 32), ylo = (u32)ny;
        asm volatile("" : "+v"(a[0][0]), "+v"(a[0][1]), "+v"(a[1][0]), "+v"(a[1][1]), "+v"(yoff), "+v"(yhi), "+v"(ylo));
        __builtin_amdgcn_s_setprio(3);
        lds_barrier();
        const u32x4 b0 = sm.ov[r], b1 = sm.ov[4 + r];
        f32x4 d0 = {0.f, 0.f, 0.f, 0.f}, d1 = {0.f, 0.f, 0.f, 0.f};
        d0 = __builtin_amdgcn_mfma_f32_16x16x32_bf16(__builtin_bit_cast(bf16x8, a[0][0]), __builtin_bit_cast(bf16x8, b0), d0, 0, 0, 0);
        d1 = __builtin_amdgcn_mfma_f32_16x16x32_bf16(__builtin_bit_cast(bf16x8, a[1][0]), __builtin_bit_cast(bf16x8, b0), d1, 0, 0, 0);
        d0 = __builtin_amdgcn_mfma_f32_16x16x32_bf16(__builtin_bit_cast(bf16x8, a[0][1]), __builtin_bit_cast(bf16x8, b1), d0, 0, 0, 0);
        d1 = __builtin_amdgcn_mfma_f32_16x16x32_bf16(__builtin_bit_cast(bf16x8, a[1][1]), __builtin_bit_cast(bf16x8, b1), d1, 0, 0, 0);
        const f32x4 d = (c & 2) ? d1 : d0;
        const float y0 = (c & 1) ? d[2] : d[0], y1 = (c & 1) ? d[3] : d[1];
        if (c < 4) st_sc1(rws, yoff, u32x4{__float_as_uint(y0), __float_as_uint(y1), yhi, ylo});
    } else {
        lds_barrier();
        if (wave == 11 && j == 0 && lane == 0) __hip_atomic_store((u64*)(ws + G_OFF + h * 256), n1, __ATOMIC_RELAXED, __HIP_MEMORY_SCOPE_AGENT);
    }
}

extern "C" void kernel_launch(void* const* d_in, const int* in_sizes, int n_in,
                              void* d_out, int out_size, void* d_ws, size_t ws_size,
                              hipStream_t stream) {
    fused<<<NWG, 1024, 0, stream>>>((const uint16_t*)d_in[0], (const uint16_t*)d_in[1], (const uint16_t*)d_in[2], (const uint16_t*)d_in[3], (const uint16_t*)d_in[4],
                                    (const uint16_t*)d_in[5], (const uint16_t*)d_in[6], (const uint16_t*)d_in[7], (const uint16_t*)d_in[8], (const uint16_t*)d_in[9],
                                    (unsigned char*)d_ws, (uint16_t*)d_out);
}
```

```cpp
#include <hip/hip_runtime.h>
#include <hip/hip_bf16.h>
#include <stdint.h>

typedef unsigned int u32;
typedef unsigned long long u64;
typedef __attribute__((ext_vector_type(4))) unsigned int u32x4;
typedef __attribute__((ext_vector_type(2))) unsigned int u32x2;
typedef __attribute__((ext_vector_type(16))) unsigned int u32x16;
typedef __attribute__((ext_vector_type(4))) float f32x4;
typedef __attribute__((ext_vector_type(2))) float f32x2;
typedef __attribute__((ext_vector_type(2))) __bf16 bf16x2;
typedef __attribute__((ext_vector_type(8))) __bf16 bf16x8;
typedef __amdgpu_buffer_rsrc_t rsrc_t;

constexpr int NH = 32, SK = 8192, DH = 128, DM = 640, HD = NH * DH;
constexpr float SCALE = 0.08838834764831845f;
constexpr float LOG2E = 1.4426950408889634f;
constexpr int NWG = 256, NW = 16;
constexpr int NL = 40;
constexpr int NRED = 160;
#define PF 8
#define PFV 6
#define FA1 0
#define FA2 12
#define FA3 24
#define PW 2
#define NPROBE 2
#define GSL2 8
#define DSL 4
#define PCAP 200
#define GSLEEP 1
#define RSLEEP 1
#define WL 1
#define WN 4
#define WP0 6
#define TL 1
#define OS 16
#define EE 0
#define ES 1
#define PB 12
#define PG 1
#define EB 2
#define EV1 0
#define E12 0
#define E03 0
#define XA 4
#define QSL 4
#define SSL 4
constexpr u32 SPOLL = 64;
constexpr u32 SPIN_MAX = 1u << 20;
constexpr u32 G_OFF = 2048;
constexpr u32 Q_OFF = 16384;
constexpr u32 R_OFF = Q_OFF + 32 * 1024;
constexpr u32 Y_OFF = R_OFF + 2 * 32 * 2 * 4096;
constexpr u32 D_OFF = Y_OFF + NRED * 64 * 64;
constexpr u32 S_OFF = D_OFF + 32 * 128;
constexpr u32 E_OFF = ES == 1 ? 0 : S_OFF + 32 * 8 * 16 * 256;
constexpr u32 WS_BYTES = S_OFF + 32 * 8 * 16 * 256 + NRED * 8 * ES;

__device__ __forceinline__ float dot2(u32 a, u32 b, float c) {
    return __builtin_amdgcn_fdot2_f32_bf16(__builtin_bit_cast(bf16x2, a), __builtin_bit_cast(bf16x2, b), c, false);
}
template <int CTRL> __device__ __forceinline__ float dpp(float x) {
    return __builtin_bit_cast(float, __builtin_amdgcn_mov_dpp(__builtin_bit_cast(int, x), CTRL, 0xf, 0xf, true));
}
constexpr int XOR1 = 0xB1, XOR2 = 0x4E, HMIR = 0x141, MIR = 0x140, ROR4 = 0x124, ROR8 = 0x128;
__device__ __forceinline__ float row16_sum(float x) {
    x += dpp<XOR1>(x); x += dpp<XOR2>(x); x += dpp<HMIR>(x); x += dpp<MIR>(x); return x;
}
__device__ __forceinline__ float row16_max(float x) {
    x = fmaxf(x, dpp<XOR1>(x)); x = fmaxf(x, dpp<XOR2>(x)); x = fmaxf(x, dpp<HMIR>(x)); x = fmaxf(x, dpp<MIR>(x)); return x;
}
__device__ __forceinline__ float sw16_sum2(float a, float b) {
    auto s = __builtin_amdgcn_permlane16_swap(__float_as_uint(a), __float_as_uint(b), false, false);
    return __uint_as_float(s[0]) + __uint_as_float(s[1]);
}
__device__ __forceinline__ float sw32_sum2(float a, float b) {
    auto t = __builtin_amdgcn_permlane32_swap(__float_as_uint(a), __float_as_uint(b), false, false);
    return __uint_as_float(t[0]) + __uint_as_float(t[1]);
}
__device__ __forceinline__ float xrow_sum(float x) { x = sw16_sum2(x, x); return sw32_sum2(x, x); }
__device__ __forceinline__ float sw32_max(float x) {
    auto t = __builtin_amdgcn_permlane32_swap(__float_as_uint(x), __float_as_uint(x), false, false);
    return fmaxf(__uint_as_float(t[0]), __uint_as_float(t[1]));
}
__device__ __forceinline__ float xrow_max(float x) {
    auto s = __builtin_amdgcn_permlane16_swap(__float_as_uint(x), __float_as_uint(x), false, false);
    return sw32_max(fmaxf(__uint_as_float(s[0]), __uint_as_float(s[1])));
}
__device__ __forceinline__ float rdlane(float x, int l) { return __builtin_bit_cast(float, __builtin_amdgcn_readlane(__builtin_bit_cast(int, x), l)); }
__device__ __forceinline__ float bf16r(float x) { return (float)(__bf16)x; }
__device__ __forceinline__ float lo16(u32 w) { return __uint_as_float(w << 16); }
__device__ __forceinline__ float hi16(u32 w) { return __uint_as_float(w & 0xffff0000u); }
__device__ __forceinline__ rsrc_t mkrsrc(const void* p, u32 bytes) { return __builtin_amdgcn_make_buffer_rsrc((void*)p, 0, bytes, 0x00020000); }
template <int AUX> __device__ __forceinline__ u32x4 bld(rsrc_t r, u32 off, u32 soff = 0) { return __builtin_amdgcn_raw_buffer_load_b128(r, off, soff, AUX); }
__device__ __forceinline__ u32x4 ld_sc1(rsrc_t r, u32 off, u32 soff = 0) { return __builtin_amdgcn_raw_buffer_load_b128(r, off, soff, 16); }
__device__ __forceinline__ void st_sc1(rsrc_t r, u32 off, u32x4 v) { __builtin_amdgcn_raw_buffer_store_b128(v, r, off, 0, 16); }
__device__ __forceinline__ void lds_barrier() { asm volatile("s_waitcnt lgkmcnt(0)\n\ts_barrier" ::: "memory"); }
__device__ __forceinline__ void lds_wait() { asm volatile("s_waitcnt lgkmcnt(0)" ::: "memory"); }
template <int N> __device__ __forceinline__ void vm_wait() { __builtin_amdgcn_s_waitcnt(0x0f70 | (N & 15) | ((N >> 4) << 14)); }
__device__ __forceinline__ u64 uni64(u64 v) { return ((u64)(u32)__builtin_amdgcn_readfirstlane((u32)(v >> 32)) << 32) | (u32)__builtin_amdgcn_readfirstlane((u32)v); }

__device__ __forceinline__ u64 sld64(const void* p) { u64 v; asm volatile("s_load_dwordx2 %0, %1, 0x0 glc\n\ts_waitcnt lgkmcnt(0)" : "=&s"(v) : "s"(p) : "memory"); return v; }
__device__ __forceinline__ u32x16 sld16(const void* p) { u32x16 v; asm volatile("s_load_dwordx16 %0, %1, 0x0 glc\n\ts_waitcnt lgkmcnt(0)" : "=&s"(v) : "s"(p) : "memory"); return v; }
__device__ __forceinline__ u32 tbad(const u32x16& d, u32 hi, u32 lo) {
    return ((d[2] ^ hi) | (d[3] ^ lo)) | ((d[6] ^ hi) | (d[7] ^ lo)) | ((d[10] ^ hi) | (d[11] ^ lo)) | ((d[14] ^ hi) | (d[15] ^ lo));
}
__device__ __forceinline__ u32 wrlane(u32 v, int l, u32 old) { asm("v_writelane_b32 %0, %1, %2" : "+v"(old) : "s"(v), "n"(l)); return old; }
__device__ __forceinline__ void glds16(const void* gsrc, u32 lds_dst) {
    u32 keep;
    asm volatile("s_mov_b32 %0, m0\n\ts_mov_b32 m0, %2\n\ts_nop 0\n\tglobal_load_lds_dwordx4 %1, off nt\n\ts_mov_b32 m0, %0" : "=&s"(keep) : "v"(gsrc), "s"(lds_dst) : "memory");
}
__device__ __forceinline__ u32 lds_ld(const u32* p) { return __builtin_amdgcn_readfirstlane(__hip_atomic_load(p, __ATOMIC_RELAXED, __HIP_MEMORY_SCOPE_WORKGROUP)); }
__device__ __forceinline__ u32 bad48(u32x4 g, u32 hi, u32 lo) { return (((g[0] ^ hi) | (g[1] ^ (hi >> 8))) & 255u) | (g[3] ^ lo); }
__device__ __forceinline__ u32 bad64(u32x4 g, u32 hi, u32 lo) { return (g[2] ^ hi) | (g[3] ^ lo); }

struct Smem {
    u32x4 wo[40 * 64];
    float part[2][NW][128];
    float ml[2][NW][2];
    u32x4 ov[8];
    float qp[4][32];
    u64 ep[48], n1;
    u32 qv[128];
    u32 qcnt, q4, cnt[2], dec, pdone, wdone;
};

__device__ __forceinline__ void combine_publish(Smem& sm, int ab, int lane, rsrc_t rws, int h, int slot, u32 nhi, u32 nlo) {
    const f32x2 ml = *(const f32x2*)&sm.ml[ab][lane & 15][0];
    f32x2 pv[NW];
#pragma unroll
    for (int w = 0; w < NW; ++w) pv[w] = *(const f32x2*)&sm.part[ab][w][2 * lane];
    const u32 off = R_OFF + (((ab * 32 + h) * 2 + (lane >> 5)) * 8 + slot) * 512 + (lane & 31) * 16;
    __builtin_amdgcn_sched_barrier(0);
    const float M = row16_max(ml[0]);
    const float f = __builtin_amdgcn_exp2f(ml[0] - M);
    const float L = row16_sum(f * ml[1]);
    f32x2 o[4];
#pragma unroll
    for (int w = 0; w < NW; ++w) { if (w < 4) o[w] = rdlane(f, w) * pv[w]; else o[w & 3] += rdlane(f, w) * pv[w]; }
    const f32x2 t = ((o[0] + o[1]) + (o[2] + o[3])) * __builtin_amdgcn_rcpf(L);
    const float W = M + __builtin_amdgcn_logf(L);
    st_sc1(rws, off, u32x4{(__float_as_uint(t[0]) & ~255u) | (nhi & 255u), (__float_as_uint(t[1]) & ~255u) | ((nhi >> 8) & 255u), __float_as_uint(W), nlo});
}

__global__ void __launch_bounds__(1024) fused(const uint16_t* __restrict__ xA, const uint16_t* __restrict__ kA, const uint16_t* __restrict__ vA,
                                              const uint16_t* __restrict__ wqA, const uint16_t* __restrict__ woA,
                                              const uint16_t* __restrict__ xB, const uint16_t* __restrict__ kB, const uint16_t* __restrict__ vB,
                                              const uint16_t* __restrict__ wqB, const uint16_t* __restrict__ woB,
                                              unsigned char* __restrict__ ws, uint16_t* __restrict__ out) {
    __shared__ __attribute__((aligned(16))) Smem sm;
    const int b = blockIdx.x;
    const int h = b >> 3, j = b & 7;
    const int tid = threadIdx.x, lane = tid & 63, wave = __builtin_amdgcn_readfirstlane(tid >> 6), r = lane >> 4, c = lane & 15;
    const int row8 = lane >> 3, c8 = lane & 7;
    const int mat = j >> 2, rh = (j >> 1) & 1, ch = j & 1;
    const rsrc_t rws = mkrsrc(ws, WS_BYTES);
    const rsrc_t rkA = mkrsrc(kA, NH * SK * DH * 2), rvA = mkrsrc(vA, NH * SK * DH * 2);
    const rsrc_t rkB = mkrsrc(kB, NH * SK * DH * 2), rvB = mkrsrc(vB, NH * SK * DH * 2);
    const u32 lo = lane * 16;
#define CLS(k) ((u32)((((k) & 1) << 14) | (((k) & 2) << 11) | (((k) & 4) << 11)))
    const u32 soB = (u32)h * (SK * 256) + ((48 + wave) << 15) + CLS(j ^ (wave & 7));
    u32x4 buf[NL];
#define GX(i) ((u32)(((i) << 19) | (((i) & 1) << 14) | (((i) >> 1) << 12)))
#define LD(n) ((n) < 8 ? bld<2>(((n) & 4) ? rvB : rkB, lo + ((n) & 3) * 1024, soB) : (n) < 24 ? bld<2>(rkA, lo + ((n) & 3) * 1024, soK ^ GX(((n) - 8) >> 2)) : bld<2>(rvA, lo + ((n) & 3) * 1024, soV ^ GX(((n) - 24) >> 2)))
    __builtin_amdgcn_s_setprio(3);
    u32 soK = 0, soV = 0;
    if (wave < 4) {
        const int hq = (h & ~1) | (j >> 2), qm = (j >> 1) & 1, qd0 = (j & 1) * 64 + (h & 1) * 32;
        const rsrc_t rw = mkrsrc(qm ? wqB : wqA, DM * HD * 2), rx = mkrsrc(qm ? xB : xA, DM * 2);
        const u32 wb = (wave * 160 * HD + hq * DH + qd0) * 2;
        const u32x4 x4 = bld<0>(rx, 320 * wave + 20 * (lane >> 2));
        const u32 x1 = __builtin_amdgcn_raw_buffer_load_b32(rx, 320 * wave + 20 * (lane >> 2) + 16, 0, 0);
        u32x4 w[10];
#pragma unroll
        for (int i = 0; i < 10; ++i) w[i] = bld<2>(rw, ((lane >> 2) * 10 * HD + (lane & 3) * 8) * 2, wb + i * HD * 2);
        __builtin_amdgcn_sched_barrier(0);
#pragma unroll
        for (int n = 0; n < E03; ++n) { buf[n] = LD(n); __builtin_amdgcn_sched_barrier(0); }
        lds_barrier();
        const u64 nq = sld64(ws + G_OFF + hq * 256) + 1;
        __builtin_amdgcn_sched_barrier(0);
        float q[8];
#pragma unroll
        for (int k = 0; k < 8; ++k) q[k] = 0.f;
#pragma unroll
        for (int i = 0; i < 10; ++i) {
            const u32 xw = i < 8 ? x4[i >> 1] : x1;
            const float xs = (i & 1) ? hi16(xw) : lo16(xw);
#pragma unroll
            for (int k = 0; k < 4; ++k) { q[2 * k] = fmaf(xs, lo16(w[i][k]), q[2 * k]); q[2 * k + 1] = fmaf(xs, hi16(w[i][k]), q[2 * k + 1]); }
        }
#pragma unroll
        for (int k = 0; k < 8; ++k) { q[k] += dpp<ROR4>(q[k]); q[k] += dpp<ROR8>(q[k]); q[k] = xrow_sum(q[k]); }
        if (lane < 4) { *(f32x4*)&sm.qp[wave][8 * lane] = f32x4{q[0], q[1], q[2], q[3]}; *(f32x4*)&sm.qp[wave][8 * lane + 4] = f32x4{q[4], q[5], q[6], q[7]}; }
        u32 old = 0;
        lds_wait();
        if (lane == 0) old = __hip_atomic_fetch_add(&sm.q4, 1u, __ATOMIC_RELAXED, __HIP_MEMORY_SCOPE_WORKGROUP);
        if (__builtin_amdgcn_readfirstlane(old) == 3) {
            asm volatile("" ::: "memory");
            const f32x4 s = (*(const f32x4*)&sm.qp[0][4 * c8] + *(const f32x4*)&sm.qp[1][4 * c8]) + (*(const f32x4*)&sm.qp[2][4 * c8] + *(const f32x4*)&sm.qp[3][4 * c8]);
            const u32 p0 = (__float_as_uint(bf16r(s[0])) >> 16) | (__float_as_uint(bf16r(s[1])) & 0xffff0000u);
            const u32 p1 = (__float_as_uint(bf16r(s[2])) >> 16) | (__float_as_uint(bf16r(s[3])) & 0xffff0000u);
            if (lane < 8) st_sc1(rws, Q_OFF + hq * 1024 + (qm * 128 + qd0) * 4 + lo, u32x4{p0, p1, (u32)(nq >> 32), (u32)nq});
        }
#pragma nounroll
        for (u32 spin = 0; spin < SPIN_MAX * 16; ++spin) {
            if (lds_ld(&sm.pdone)) break;
            __builtin_amdgcn_s_sleep(2);
        }
        asm volatile("" ::: "memory");
        {
            const u64 n1 = uni64(sm.n1);
            const u32 nhi = (u32)(n1 >> 32), nlo = (u32)n1;
            const u32 qb = __builtin_amdgcn_readfirstlane(Q_OFF + h * 1024 + wave * 256);
            bool ok = false;
            u32x16 d0, d1, d2, d3;
#pragma nounroll
            for (u32 spin = 0; spin < SPOLL; ++spin) {
                asm volatile("s_load_dwordx16 %0, %4, 0x0 glc\n\ts_load_dwordx16 %1, %4, 0x40 glc\n\ts_load_dwordx16 %2, %4, 0x80 glc\n\ts_load_dwordx16 %3, %4, 0xc0 glc\n\ts_waitcnt lgkmcnt(0)"
                             : "=&s"(d0), "=&s"(d1), "=&s"(d2), "=&s"(d3) : "s"(ws + qb) : "memory");
                if (((tbad(d0, nhi, nlo) | tbad(d1, nhi, nlo)) | (tbad(d2, nhi, nlo) | tbad(d3, nhi, nlo))) == 0) { ok = true; break; }
                __builtin_amdgcn_s_sleep(QSL);
            }
            u32 g0 = 0, g1 = 0;
            if (ok) {
#pragma unroll
                for (int i = 0; i < 4; ++i) {
                    g0 = wrlane(d0[4 * i], i, g0); g1 = wrlane(d0[4 * i + 1], i, g1);
                    g0 = wrlane(d1[4 * i], 4 + i, g0); g1 = wrlane(d1[4 * i + 1], 4 + i, g1);
                    g0 = wrlane(d2[4 * i], 8 + i, g0); g1 = wrlane(d2[4 * i + 1], 8 + i, g1);
                    g0 = wrlane(d3[4 * i], 12 + i, g0); g1 = wrlane(d3[4 * i + 1], 12 + i, g1);
                }
            } else {
                u32 off = qb + c * 16;
                u32x4 g;
#pragma nounroll
                for (u32 spin = 0; spin < SPIN_MAX; ++spin) {
                    asm volatile("" : "+v"(off));
                    g = ld_sc1(rws, off);
                    if (__all(bad64(g, nhi, nlo) == 0)) break;
                    __builtin_amdgcn_s_sleep(1);
                }
                g0 = g[0]; g1 = g[1];
            }
            if (lane < 16) *(u32x2*)&sm.qv[wave * 32 + 2 * lane] = u32x2{g0, g1};
            lds_wait();
            if (lane == 0) __hip_atomic_fetch_add(&sm.qcnt, 1u, __ATOMIC_RELAXED, __HIP_MEMORY_SCOPE_WORKGROUP);
        }
        __builtin_amdgcn_sched_barrier(0);
#pragma unroll
        for (int n = E03; n < 8; ++n) {
            if (n >= PW + E03 && lds_ld(&sm.dec) == 0) vm_wait<PW - 1>();
            buf[n] = LD(n);
            __builtin_amdgcn_sched_barrier(0);
        }
    } else if (wave == 12) {
#pragma unroll
        for (int n = 0; n < E12; ++n) { buf[n] = LD(n); __builtin_amdgcn_sched_barrier(0); }
        if (lane == 0) { sm.qcnt = 0; sm.q4 = 0; sm.cnt[0] = 0; sm.cnt[1] = 0; sm.dec = 0; sm.pdone = 0; sm.wdone = 0; }
        lds_barrier();
        int gs = 0;
        {
            const unsigned char* pa = (const unsigned char*)((j & 1) ? vA : kA) + (size_t)h * (SK * 256) + (j >> 1) * (16 * 32768);
#pragma unroll
            for (int i = 0; i < NPROBE; ++i) {
                const int side = ((j >> 1) ^ i) & 1;
                const unsigned char* p1 = pa + i * 32768 + side * 8192;
                const unsigned char* p2 = pa + i * 32768 + (side ^ 1) * 8192;
                u32 v1, v2; u64 t1, t2;
                asm volatile("s_load_dword %0, %4, 0x0 glc\n\ts_load_dword %1, %5, 0x0 glc\n\ts_waitcnt lgkmcnt(1)\n\ts_memtime %2\n\ts_waitcnt lgkmcnt(0)\n\ts_memtime %3\n\ts_waitcnt lgkmcnt(0)"
                             : "=&s"(v1), "=&s"(v2), "=&s"(t1), "=&s"(t2) : "s"(p1), "s"(p2) : "memory");
                const int g = min((int)(u32)(t2 - t1), PCAP);
                gs += side ? -g : g;
            }
        }
        u64 e1;
        {
            u32 dummy;
            asm volatile("s_load_dword %1, %3, 0x0\n\ts_load_dwordx2 %0, %2, 0x0 glc\n\ts_waitcnt lgkmcnt(0)" : "=&s"(e1), "=&s"(dummy)
                         : "s"(ws + G_OFF + h * 256), "s"((const unsigned char*)((j & 1) ? kA : vA) + (size_t)h * (SK * 256) + j * 4096) : "memory");
        }
        const u64 n1 = e1 + 1;
        const u32 nhi = (u32)(n1 >> 32), nlo = (u32)n1;
        const int iod0 = (__builtin_amdgcn_s_getreg((20 << 0) | (0 << 6) | (3 << 11)) >> 2) & 1;
        if (lane == 0) st_sc1(rws, D_OFF + h * 128 + j * 16, u32x4{(u32)(iod0 ? -gs : gs), (u32)iod0, nhi, nlo});
        if (lane == 0) { sm.n1 = n1; lds_wait(); __hip_atomic_store(&sm.pdone, 1u, __ATOMIC_RELAXED, __HIP_MEMORY_SCOPE_WORKGROUP); }
        int vg[8], vi[8];
        bool ok = false;
#pragma nounroll
        for (u32 spin = 0; spin < SPOLL; ++spin) {
            __builtin_amdgcn_s_sleep(2);
            u32x16 d0, d1;
            asm volatile("s_load_dwordx16 %0, %2, 0x0 glc\n\ts_load_dwordx16 %1, %2, 0x40 glc\n\ts_waitcnt lgkmcnt(0)" : "=&s"(d0), "=&s"(d1) : "s"(ws + (u32)__builtin_amdgcn_readfirstlane(D_OFF + h * 128)) : "memory");
#pragma unroll
            for (int i = 0; i < 4; ++i) { vg[i] = d0[4 * i]; vi[i] = d0[4 * i + 1]; vg[4 + i] = d1[4 * i]; vi[4 + i] = d1[4 * i + 1]; }
            if ((tbad(d0, nhi, nlo) | tbad(d1, nhi, nlo)) == 0) { ok = true; break; }
        }
        if (!ok) {
            u32 off = D_OFF + h * 128 + c8 * 16;
            u32x4 g;
#pragma nounroll
            for (u32 spin = 0; spin < SPIN_MAX; ++spin) {
                asm volatile("" : "+v"(off));
                g = ld_sc1(rws, off);
                if (__all(bad64(g, nhi, nlo) == 0)) break;
                __builtin_amdgcn_s_sleep(1);
            }
#pragma unroll
            for (int i = 0; i < 8; ++i) { vg[i] = __builtin_amdgcn_readlane(g[0], i); vi[i] = __builtin_amdgcn_readlane(g[1], i); }
        }
        int msk = 0;
#pragma unroll
        for (int i = 0; i < 8; ++i) msk |= (vi[i] & 1) << i;
        const int sk = (vg[0] + vg[2]) + (vg[4] + vg[6]), sv = (vg[1] + vg[3]) + (vg[5] + vg[7]);
        const int iod = (msk >> j) & 1;
        const int rank = __builtin_popcount((iod ? msk : ~msk) & ((1 << j) - 1));
        const int pk_ = sk > 0 ? 0 : 1, pv_ = sv > 0 ? 0 : 1;
        const bool bal = __builtin_popcount(msk) == 4;
        const u32 d = 0x100u | (bal ? ((iod ^ pk_) * 4 + rank) | (((iod ^ pv_) * 4 + rank) << 4) : j * 17);
        if (lane == 0) __hip_atomic_store(&sm.dec, d, __ATOMIC_RELAXED, __HIP_MEMORY_SCOPE_WORKGROUP);
        __builtin_amdgcn_sched_barrier(0);
#pragma unroll
        for (int n = E12; n < 8; ++n) { buf[n] = LD(n); __builtin_amdgcn_sched_barrier(0); }
    } else {
#pragma unroll
        for (int n = 0; n < EB; ++n) { buf[n] = LD(n); __builtin_amdgcn_sched_barrier(0); }
        lds_barrier();
        __builtin_amdgcn_sched_barrier(0);
        u64 ev = 0;
        if (!EV1) ev = __hip_atomic_load((const u64*)(ws + E_OFF) + (lane < 40 ? mat * 80 + ch * 40 + lane : b < NRED ? b : 0) * ES, __ATOMIC_RELAXED, __HIP_MEMORY_SCOPE_AGENT);
#pragma unroll
        for (int n = EB; n < 8; ++n) {
            if (lds_ld(&sm.dec) == 0) vm_wait<PW - 1>();
            buf[n] = LD(n);
            __builtin_amdgcn_sched_barrier(0);
        }
        if (EV1 && wave == 8) ev = __hip_atomic_load((const u64*)(ws + E_OFF) + (lane < 40 ? mat * 80 + ch * 40 + lane : b < NRED ? b : 0) * ES, __ATOMIC_RELAXED, __HIP_MEMORY_SCOPE_AGENT);
        if (wave == 8 && lane < 48) sm.ep[lane] = ev + 1;
    }
    __builtin_amdgcn_sched_barrier(0);
    u32 dec = 0;
#pragma nounroll
    for (u32 spin = 0; spin < SPIN_MAX * 16; ++spin) {
        dec = lds_ld(&sm.dec);
        if (dec) break;
        __builtin_amdgcn_s_sleep(DSL);
    }
    const int kK = dec & 7, kV = (dec >> 4) & 7;
    soK = (u32)h * (SK * 256) + (wave << 15) + CLS(kK);
    soV = (u32)h * (SK * 256) + (wave << 15) + CLS(kV);
    __builtin_amdgcn_sched_barrier(0);
#pragma unroll
    for (int n = 8; n < 8 + PF; ++n) { buf[n] = LD(n); __builtin_amdgcn_sched_barrier(0); }
    __builtin_amdgcn_sched_barrier(0);
#pragma nounroll
    for (u32 spin = 0; spin < SPIN_MAX * 16; ++spin) {
        if (lds_ld(&sm.qcnt) == 4) break;
        __builtin_amdgcn_s_sleep(DSL);
    }
    asm volatile("" ::: "memory");
    const u32x4 qa4 = *(const u32x4*)&sm.qv[4 * c], qb4 = *(const u32x4*)&sm.qv[64 + 4 * c];
    const u64 n1 = uni64(sm.n1);
    const u32 nhi = (u32)(n1 >> 32), nlo = (u32)n1;
    float acc[8], m = -INFINITY, l = 0.f, sc[4];
#pragma unroll
    for (int k = 0; k < 8; ++k) acc[k] = 0.f;
#define FLUSH(AB) do { const float t0_ = sw32_sum2(acc[0], acc[4]), t1_ = sw32_sum2(acc[1], acc[5]), t2_ = sw32_sum2(acc[2], acc[6]), t3_ = sw32_sum2(acc[3], acc[7]); \
        *(f32x2*)&sm.part[AB][wave][8 * c + 2 * r] = f32x2{sw16_sum2(t0_, t2_), sw16_sum2(t1_, t3_)}; } while (0)
#define PUTML(AB) do { l = xrow_sum(l); if (lane == 0) *(f32x2*)&sm.ml[AB][wave][0] = f32x2{m, l}; } while (0)
#define TS(n) ((n) < 8 ? (n) + 16 : (n) < 24 ? (n) - 8 : (n))
#define NEXT(n) do { __builtin_amdgcn_sched_barrier(0); \
        if (TS(n) == FA1) __builtin_amdgcn_s_setprio(2); if (TS(n) == FA2) __builtin_amdgcn_s_setprio(1); if (TS(n) == FA3) __builtin_amdgcn_s_setprio(0); \
        if ((n) >= 8 && (n) + PF < 24) buf[(n) + PF] = LD((n) + PF); \
        if (TS(n) + PFV >= 24 && TS(n) + PFV < NL) buf[TS(n) + PFV] = LD(TS(n) + PFV); \
        __builtin_amdgcn_sched_barrier(0); } while (0)
#define SCORE(B, Q) ({ float d_ = dot2((B)[0], (Q)[0], 0.f); d_ = dot2((B)[1], (Q)[1], d_); d_ = dot2((B)[2], (Q)[2], d_); d_ = dot2((B)[3], (Q)[3], d_); \
                       d_ = row16_sum(d_); bf16r(bf16r(d_) * SCALE); })
#define PV(B, P) do { _Pragma("unroll") for (int k = 0; k < 4; ++k) { acc[2 * k] = fmaf((P), lo16((B)[k]), acc[2 * k]); acc[2 * k + 1] = fmaf((P), hi16((B)[k]), acc[2 * k + 1]); } \
        asm volatile("" : "+v"(acc[0]), "+v"(acc[1]), "+v"(acc[2]), "+v"(acc[3]), "+v"(acc[4]), "+v"(acc[5]), "+v"(acc[6]), "+v"(acc[7]) :: "memory"); } while (0)
    const bool xch = kK != kV;
    u32 pk[8];
#pragma unroll
    for (int n = 8; n < 24; ++n) {
        const u32 sb = __float_as_uint(SCORE(buf[n], qa4));
        if (n & 1) pk[(n - 8) >> 1] |= sb & 0xffff0000u; else pk[(n - 8) >> 1] = sb >> 16;
        asm volatile("" : "+v"(pk[(n - 8) >> 1]) :: "memory");
        if (n == 23 && xch) {
            u32 w0 = pk[0], w1 = pk[1];
#pragma unroll
            for (int k = 1; k < 4; ++k) { w0 = c == k ? pk[2 * k] : w0; w1 = c == k ? pk[2 * k + 1] : w1; }
            if (c < 4) st_sc1(rws, S_OFF + ((h * 8 + kK) * 16 + wave) * 256 + c * 64 + r * 16, u32x4{w0, w1, nhi, nlo});
        }
        NEXT(n);
    }
#pragma unroll
    for (int n = 0; n < 4; ++n) { sc[n] = SCORE(buf[n], qb4) * LOG2E; asm volatile("" : "+v"(sc[n]) :: "memory"); NEXT(n); }
    m = xrow_max(fmaxf(fmaxf(sc[0], sc[1]), fmaxf(sc[2], sc[3])));
#pragma unroll
    for (int n = 4; n < 8; ++n) { const float p = __builtin_amdgcn_exp2f(sc[n & 3] - m); l += p; PV(buf[n], p); NEXT(n); }
    {
        PUTML(1);
        FLUSH(1);
        u32 old = 0;
        lds_wait();
        if (lane == 0) old = __hip_atomic_fetch_add(&sm.cnt[1], 1u, __ATOMIC_RELAXED, __HIP_MEMORY_SCOPE_WORKGROUP);
        if (PB < 0 && __builtin_amdgcn_readfirstlane(old) == NW - 1) { asm volatile("" ::: "memory"); combine_publish(sm, 1, lane, rws, h, j, nhi, nlo); }
        l = 0.f;
#pragma unroll
        for (int k = 0; k < 8; ++k) acc[k] = 0.f;
        __builtin_amdgcn_sched_barrier(0);
    }
    const u32 sb = __builtin_amdgcn_readfirstlane(S_OFF + ((h * 8 + kV) * 16 + wave) * 256);
#define VPHASE(I0) do { \
        m = xrow_max(fmaxf(fmaxf(fmaxf(fmaxf(lo16(pk[0]), hi16(pk[0])), fmaxf(lo16(pk[1]), hi16(pk[1]))), fmaxf(fmaxf(lo16(pk[2]), hi16(pk[2])), fmaxf(lo16(pk[3]), hi16(pk[3])))), \
                           fmaxf(fmaxf(fmaxf(lo16(pk[4]), hi16(pk[4])), fmaxf(lo16(pk[5]), hi16(pk[5]))), fmaxf(fmaxf(lo16(pk[6]), hi16(pk[6])), fmaxf(lo16(pk[7]), hi16(pk[7])))))) * LOG2E; \
        _Pragma("unroll") for (int i = 0; i < 4; ++i) { \
            const u32 s0 = pk[2 * i], s1 = pk[2 * i + 1]; \
            sc[0] = __builtin_amdgcn_exp2f(lo16(s0) * LOG2E - m); sc[1] = __builtin_amdgcn_exp2f(hi16(s0) * LOG2E - m); \
            sc[2] = __builtin_amdgcn_exp2f(lo16(s1) * LOG2E - m); sc[3] = __builtin_amdgcn_exp2f(hi16(s1) * LOG2E - m); \
            l += (sc[0] + sc[1]) + (sc[2] + sc[3]); \
            if (i == 3) PUTML(0); \
            __builtin_amdgcn_sched_barrier(0); \
            _Pragma("unroll") for (int n = 24 + 4 * i; n < 28 + 4 * i; ++n) { PV(buf[n], sc[n & 3]); __builtin_amdgcn_sched_barrier(0); \
                if (n == FA3) __builtin_amdgcn_s_setprio(0); \
                if (n + PFV >= (I0) && n + PFV < NL) buf[n + PFV] = LD(n + PFV); \
                __builtin_amdgcn_sched_barrier(0); } \
        } \
        FLUSH(0); } while (0)
#define SPREAD(D) do { _Pragma("unroll") for (int i = 0; i < 4; ++i) { \
            _Pragma("unroll") for (int k = 0; k < 4; ++k) { pk[2 * i] = wrlane((D)[i][4 * k], 16 * k, pk[2 * i]); pk[2 * i + 1] = wrlane((D)[i][4 * k + 1], 16 * k, pk[2 * i + 1]); } } \
        asm volatile("s_nop 1" : "+v"(pk[6]), "+v"(pk[7])); \
        _Pragma("unroll") for (int k = 0; k < 8; ++k) pk[k] = __builtin_amdgcn_mov_dpp(pk[k], 0x150, 0xf, 0xf, true); } while (0)
#define SLD4(D) asm volatile("s_load_dwordx16 %0, %4, 0x0 glc\n\ts_load_dwordx16 %1, %4, 0x40 glc\n\ts_load_dwordx16 %2, %4, 0x80 glc\n\ts_load_dwordx16 %3, %4, 0xc0 glc\n\ts_waitcnt lgkmcnt(0)" \
                     : "=&s"((D)[0]), "=&s"((D)[1]), "=&s"((D)[2]), "=&s"((D)[3]) : "s"(ws + sb) : "memory")
#define TB4(D) ((tbad((D)[0], nhi, nlo) | tbad((D)[1], nhi, nlo)) | (tbad((D)[2], nhi, nlo) | tbad((D)[3], nhi, nlo)))
    bool late = false;
    if (xch) {
        u32x16 d[4];
        SLD4(d);
        late = TB4(d) != 0;
        if (!late) SPREAD(d);
    }
    if (late) {
#pragma unroll
        for (int n = 24 + PFV; n < 24 + PFV + XA; ++n) { buf[n] = LD(n); __builtin_amdgcn_sched_barrier(0); }
        bool ok = false;
        u32x16 d[4];
#pragma nounroll
        for (u32 spin = 0; spin < SPOLL; ++spin) {
            SLD4(d);
            if (TB4(d) == 0) { ok = true; break; }
            __builtin_amdgcn_s_sleep(SSL);
        }
        if (ok) SPREAD(d);
        else {
            u32 soff = sb + (c & 3) * 64 + r * 16;
            u32x4 g;
#pragma nounroll
            for (u32 spin = 0; spin < SPIN_MAX; ++spin) {
                asm volatile("" : "+v"(soff));
                g = ld_sc1(rws, soff);
                if (__all(bad64(g, nhi, nlo) == 0)) break;
                __builtin_amdgcn_s_sleep(1);
            }
            pk[0] = __builtin_amdgcn_mov_dpp(g[0], 0x150, 0xf, 0xf, true); pk[1] = __builtin_amdgcn_mov_dpp(g[1], 0x150, 0xf, 0xf, true);
            pk[2] = __builtin_amdgcn_mov_dpp(g[0], 0x151, 0xf, 0xf, true); pk[3] = __builtin_amdgcn_mov_dpp(g[1], 0x151, 0xf, 0xf, true);
            pk[4] = __builtin_amdgcn_mov_dpp(g[0], 0x152, 0xf, 0xf, true); pk[5] = __builtin_amdgcn_mov_dpp(g[1], 0x152, 0xf, 0xf, true);
            pk[6] = __builtin_amdgcn_mov_dpp(g[0], 0x153, 0xf, 0xf, true); pk[7] = __builtin_amdgcn_mov_dpp(g[1], 0x153, 0xf, 0xf, true);
        }
        if (XA) VPHASE(24 + PFV + XA);
    }
    if (!late || !XA) VPHASE(24 + PFV);
    {
        u32 old = 0;
        lds_wait();
        if (lane == 0) old = __hip_atomic_fetch_add(&sm.cnt[0], wave == 5 ? 1u : 0x101u, __ATOMIC_RELAXED, __HIP_MEMORY_SCOPE_WORKGROUP);
        old = __builtin_amdgcn_readfirstlane(old);
        if ((old & 255) == NW - 1) {
            asm volatile("" ::: "memory"); __builtin_amdgcn_s_setprio(3); combine_publish(sm, 0, lane, rws, h, kV, nhi, nlo);
        }
        if (PB >= 0 && (old & 255) == PB) {
#pragma nounroll
            for (u32 spin = 0; spin < SPIN_MAX * 16; ++spin) {
                if (lds_ld(&sm.cnt[1]) >= NW) break;
                __builtin_amdgcn_s_sleep(GSL2);
            }
            asm volatile("" ::: "memory");
            combine_publish(sm, 1, lane, rws, h, j, nhi, nlo);
        }
        const u32 pos = (old >> 8) - WP0;
        if (WL && wave != 5 && pos < WN) {
            const uint16_t* wsrc = (mat ? woB : woA) + (size_t)(h * DH + rh * 64 + row8) * DM + ch * 320 + c8 * 8;
#pragma unroll
            for (int i = 0; i < 40 / WN; ++i) {
                const int idx = pos * (40 / WN) + i;
                glds16(wsrc + (idx & 7) * 8 * DM + (idx >> 3) * 64, (u32)(uintptr_t)&sm.wo[idx * 64]);
            }
            vm_wait<0>();
            if (lane == 0) __hip_atomic_fetch_add(&sm.wdone, 1u, __ATOMIC_RELAXED, __HIP_MEMORY_SCOPE_WORKGROUP);
        }
    }
    if (wave == 5) {
#pragma nounroll
        for (u32 spin = 0; spin < SPIN_MAX * 16; ++spin) {
            if (PB >= 0 && PG ? (lds_ld(&sm.cnt[0]) & 255) >= (mat ? PB + 1 : NW) : (lds_ld(&sm.cnt[mat]) & 255) >= NW) break;
            __builtin_amdgcn_s_sleep(GSL2);
        }
        __builtin_amdgcn_s_setprio(3);
        u32 off = R_OFF + ((mat * 32 + h) * 2 + rh) * 4096 + lo;
        u32x4 g[4];
#pragma unroll
        for (int i = 0; i < 4; ++i) g[i] = ld_sc1(rws, off + i * 1024);
#pragma nounroll
        for (u32 spin = 0; spin < SPIN_MAX; ++spin) {
            bool p[4];
#pragma unroll
            for (int i = 0; i < 4; ++i) p[i] = bad48(g[i], nhi, nlo) != 0;
            if (!__any(p[0] | p[1] | p[2] | p[3])) break;
            if (GSLEEP) __builtin_amdgcn_s_sleep(GSLEEP);
            asm volatile("" : "+v"(off));
#pragma unroll
            for (int i = 0; i < 4; ++i) if (p[i]) g[i] = ld_sc1(rws, off + i * 1024);
        }
        const float W = sw32_max(fmaxf(fmaxf(__uint_as_float(g[0][2]), __uint_as_float(g[1][2])), fmaxf(__uint_as_float(g[2][2]), __uint_as_float(g[3][2]))));
        float o0 = 0.f, o1 = 0.f, S = 0.f;
#pragma unroll
        for (int i = 0; i < 4; ++i) {
            const float e = __builtin_amdgcn_exp2f(__uint_as_float(g[i][2]) - W);
            S += e; o0 = fmaf(e, __uint_as_float(g[i][0] & ~255u), o0); o1 = fmaf(e, __uint_as_float(g[i][1] & ~255u), o1);
        }
        const float o = sw32_sum2(o0, o1) * __builtin_amdgcn_rcpf(sw32_sum2(S, S));
        ((uint16_t*)sm.ov)[2 * (lane & 31) + (lane >> 5)] = (uint16_t)(__float_as_uint(bf16r(o)) >> 16);
        lds_barrier();
        if (b < NRED) {
            const u64 nr = uni64(sm.ep[40]);
            const u32 rhi = (u32)(nr >> 32), rlo = (u32)nr;
            constexpr u32 YS = 1024;
            off = Y_OFF + b * 4096 + lo;
#pragma unroll
            for (int i = 0; i < 4; ++i) g[i] = ld_sc1(rws, off + i * YS);
#pragma nounroll
            for (u32 spin = 0; spin < SPIN_MAX; ++spin) {
                bool p[4];
#pragma unroll
                for (int i = 0; i < 4; ++i) p[i] = bad64(g[i], rhi, rlo) != 0;
                if (!__any(p[0] | p[1] | p[2] | p[3])) break;
                if (RSLEEP) __builtin_amdgcn_s_sleep(RSLEEP);
                asm volatile("" : "+v"(off));
#pragma unroll
                for (int i = 0; i < 4; ++i) if (p[i]) g[i] = ld_sc1(rws, off + i * YS);
            }
            if (EE && lane == 0) __hip_atomic_store((u64*)(ws + E_OFF) + b * ES, nr, __ATOMIC_RELAXED, __HIP_MEMORY_SCOPE_AGENT);
            __builtin_amdgcn_sched_barrier(0);
            const float t0 = (__uint_as_float(g[0][0]) + __uint_as_float(g[1][0])) + (__uint_as_float(g[2][0]) + __uint_as_float(g[3][0]));
            const float t1 = (__uint_as_float(g[0][1]) + __uint_as_float(g[1][1])) + (__uint_as_float(g[2][1]) + __uint_as_float(g[3][1]));
            float t = sw32_sum2(t0, t1);
            t = sw16_sum2(t, t); t += dpp<ROR8>(t); t += dpp<ROR4>(t);
            if ((lane & 28) == 0) __builtin_amdgcn_raw_buffer_store_b16((uint16_t)(__float_as_uint(bf16r(t)) >> 16), mkrsrc(out, 2 * 2 * DM), (b * 8 + 2 * (lane & 3) + (lane >> 5)) * 2, 0, OS);
            if (!EE && lane == 0) __hip_atomic_store((u64*)(ws + E_OFF) + b * ES, nr, __ATOMIC_RELAXED, __HIP_MEMORY_SCOPE_AGENT);
        }
    } else if (wave < 11) {
        const int tp = wave < 5 ? wave : wave - 1;
#pragma nounroll
        for (u32 spin = 0; spin < SPIN_MAX * 16; ++spin) {
            if ((!WL || lds_ld(&sm.wdone) >= WN) && lds_ld(&sm.cnt[1]) >= NW) break;
            __builtin_amdgcn_s_sleep(WL ? 2 : GSL2);
        }
        asm volatile("" ::: "memory");
        u32x4 a[2][2];
        const uint16_t* wl = (const uint16_t*)&sm.wo[((tp >> 1) * 8 + r) * 64 + (tp & 1) * 4 + (c >> 3)] + c8;
#pragma unroll
        for (int t = 0; t < 2; ++t)
#pragma unroll
            for (int ks = 0; ks < 2; ++ks)
#pragma unroll
                for (int e = 0; e < 4; ++e) a[t][ks][e] = (u32)wl[(ks * 256 + 2 * e * 8 + t * 2) * 8] | ((u32)wl[(ks * 256 + (2 * e + 1) * 8 + t * 2) * 8] << 16);
        const int red = tp * 4 + (c & 2) + (r >> 1);
        const u64 ny = sm.ep[red < 40 ? red : 0];
        u32 yoff = Y_OFF + ((mat * 80 + ch * 40 + red) * 64 + h * 2 + rh) * 64 + ((r & 1) * 2 + (c & 1)) * 16;
        u32 yhi = (u32)(ny >> 32), ylo = (u32)ny;
        asm volatile("" : "+v"(a[0][0]), "+v"(a[0][1]), "+v"(a[1][0]), "+v"(a[1][1]), "+v"(yoff), "+v"(yhi), "+v"(ylo));
        __builtin_amdgcn_s_setprio(3);
        lds_barrier();
        const u32x4 b0 = sm.ov[r], b1 = sm.ov[4 + r];
        f32x4 d0 = {0.f, 0.f, 0.f, 0.f}, d1 = {0.f, 0.f, 0.f, 0.f};
        d0 = __builtin_amdgcn_mfma_f32_16x16x32_bf16(__builtin_bit_cast(bf16x8, a[0][0]), __builtin_bit_cast(bf16x8, b0), d0, 0, 0, 0);
        d1 = __builtin_amdgcn_mfma_f32_16x16x32_bf16(__builtin_bit_cast(bf16x8, a[1][0]), __builtin_bit_cast(bf16x8, b0), d1, 0, 0, 0);
        d0 = __builtin_amdgcn_mfma_f32_16x16x32_bf16(__builtin_bit_cast(bf16x8, a[0][1]), __builtin_bit_cast(bf16x8, b1), d0, 0, 0, 0);
        d1 = __builtin_amdgcn_mfma_f32_16x16x32_bf16(__builtin_bit_cast(bf16x8, a[1][1]), __builtin_bit_cast(bf16x8, b1), d1, 0, 0, 0);
        const f32x4 d = (c & 2) ? d1 : d0;
        const float y0 = (c & 1) ? d[2] : d[0], y1 = (c & 1) ? d[3] : d[1];
        if (c < 4) st_sc1(rws, yoff, u32x4{__float_as_uint(y0), __float_as_uint(y1), yhi, ylo});
    } else {
        lds_barrier();
        if (wave == 11 && j == 0 && lane == 0) __hip_atomic_store((u64*)(ws + G_OFF + h * 256), n1, __ATOMIC_RELAXED, __HIP_MEMORY_SCOPE_AGENT);
    }
}

extern "C" void kernel_launch(void* const* d_in, const int* in_sizes, int n_in,
                              void* d_out, int out_size, void* d_ws, size_t ws_size,
                              hipStream_t stream) {
    fused<<<NWG, 1024, 0, stream>>>((const uint16_t*)d_in[0], (const uint16_t*)d_in[1], (const uint16_t*)d_in[2], (const uint16_t*)d_in[3], (const uint16_t*)d_in[4],
                                    (const uint16_t*)d_in[5], (const uint16_t*)d_in[6], (const uint16_t*)d_in[7], (const uint16_t*)d_in[8], (const uint16_t*)d_in[9],
                                    (unsigned char*)d_ws, (uint16_t*)d_out);
}
```
